```python
import torch

def _softmax(x, axis=-1):
    return torch.softmax(x, dim=axis)

def _gelu(x):
    return torch.nn.functional.gelu(x, approximate='tanh')

def _silu(x):
    return torch.nn.functional.silu(x)

def _sigmoid(x):
    return torch.sigmoid(x)

def _layernorm(x, axis=-1, eps=1e-05):
    m = x.mean(dim=axis, keepdim=True)
    v = x.var(dim=axis, keepdim=True, unbiased=False)
    return (x - m) / torch.sqrt(v + eps)

def _scatter_add(tgt, idx, src, axis=0):
    return tgt.clone().index_add_(axis, idx, src)

def _segment_sum(src, seg, n):
    out = torch.zeros((n,) + src.shape[1:], dtype=src.dtype)
    return out.index_add_(0, seg, src)

def _pad(x, pw):
    flat = tuple((v for pair in reversed(pw) for v in pair))
    return torch.nn.functional.pad(x, flat)
kernel_name = 'adaLN_cond__spectral_gfnet__glu_gated_reglu__prompt'

def setup_inputs():

    _w = lambda a: (lambda t: t.to(torch.bfloat16) if t.is_floating_point() else t)(torch.as_tensor(a))

    def _A_setup():
        B, S, D = (1, 2048, 448)
        Df = D // 2 + 1
        return {'x': _w((lambda *s: torch.randn(*s, dtype=torch.bfloat16) * 0.05)(B, S, D)), 'w_r': _w((lambda *s: torch.randn(*s, dtype=torch.bfloat16) * 3.2)(S, Df) * 0.02), 'w_i': _w((lambda *s: torch.randn(*s, dtype=torch.bfloat16) * 3.2)(S, Df) * 0.02)}
    _A_in = _A_setup()

    def _B_setup():
        B, S, D, F = (1, 2048, 448, 1792)
        return {'x': _w((lambda *s: torch.randn(*s, dtype=torch.bfloat16) * 0.05)(B, S, D)), 'W_gate': _w((lambda *s: torch.randn(*s, dtype=torch.bfloat16) * 0.4)(D, F) * (2.0 / D) ** 0.5), 'W_up': _w((lambda *s: torch.randn(*s, dtype=torch.bfloat16) * 0.4)(D, F) * (2.0 / D) ** 0.5), 'W_down': _w((lambda *s: torch.randn(*s, dtype=torch.bfloat16) * 0.4)(F, D) * (1.0 / F) ** 0.5)}
    _B_in = _B_setup()
    return {'A_x': _A_in['x'], 'A_w_r': _A_in['w_r'], 'A_w_i': _A_in['w_i'], 'B_x': _B_in['x'], 'B_W_gate': _B_in['W_gate'], 'B_W_up': _B_in['W_up'], 'B_W_down': _B_in['W_down'], 'c': _w((lambda *s: torch.randn(*s, dtype=torch.bfloat16) * 0.1)(1, 448) * 8.0), 'w_mod': _w((lambda *s: torch.randn(*s, dtype=torch.bfloat16) * 0.1)(448, 6 * 448) * 448 ** (-0.5) * 8.0)}

def reference(A_x, A_w_r, A_w_i, B_x, B_W_gate, B_W_up, B_W_down, c, w_mod):

    def _A_ref(x, w_r, w_i):
        B, S, D = x.shape
        X = (lambda x, axes=(-2, -1): torch.fft.rfft2(x.float(), dim=axes))(x, axes=(-2, -1))
        W = w_r + 1j * w_i
        X = X * W
        y = (lambda x, s, axes=(-2, -1): torch.fft.irfft2(x, s=s, dim=axes).to(torch.bfloat16))(X, (S, D), axes=(-2, -1))
        return y

    def _B_ref(x, W_gate, W_up, W_down):
        gate = torch.einsum('bsd,df->bsf', x, W_gate)
        up = torch.einsum('bsd,df->bsf', x, W_up)
        gate = gate * (lambda x, dt: x.to(dt))(gate > 0, gate.dtype)
        h = gate * up
        return torch.einsum('bsf,fd->bsd', h, W_down)
    x = A_x
    mod = (_silu(c) @ w_mod).reshape(c.shape[0], 6, 1, -1)
    sa, ca, ga, sb, cb, gb = (mod[:, 0].clone(), mod[:, 1].clone(), mod[:, 2].clone(), mod[:, 3].clone(), mod[:, 4].clone(), mod[:, 5].clone())
    h = _layernorm(x, axis=-1) * (1 + ca) + sa
    x = x + ga * _A_ref(h, A_w_r, A_w_i)
    h = _layernorm(x, axis=-1) * (1 + cb) + sb
    x = x + gb * _B_ref(h, B_W_gate, B_W_up, B_W_down)
    return x
```

```cpp
#include <hip/hip_runtime.h>
#include <hip/hip_bf16.h>

typedef float cf __attribute__((ext_vector_type(2)));
typedef float f32x4 __attribute__((ext_vector_type(4)));
typedef unsigned v4u __attribute__((ext_vector_type(4)));
typedef unsigned v2u __attribute__((ext_vector_type(2)));
typedef short bf16x8 __attribute__((ext_vector_type(8)));
typedef __bf16 bf2 __attribute__((ext_vector_type(2)));
typedef __amdgpu_buffer_rsrc_t rsrc_t;
#define FMA2 __builtin_elementwise_fma
#define LAS __attribute__((address_space(3)))
#define DEVI __device__ __forceinline__

constexpr int S_ = 2048, D_ = 448, F_ = 1792, KD = 225;
constexpr int OFF_MOD = 0;
constexpr int OFF_WT = 16384;
constexpr int OFF_Z1 = OFF_WT + KD * 8192;
constexpr int OFF_Z2 = OFF_Z1 + KD * 8192;
constexpr int OFF_X1 = OFF_Z2 + KD * 8192;
constexpr int OFF_H2 = OFF_X1 + S_ * D_ * 2;
constexpr int OFF_H = OFF_H2 + S_ * D_ * 2;
constexpr int WS_BYTES = OFF_H + S_ * F_ * 2;

DEVI unsigned f2u(float v) { return __builtin_bit_cast(unsigned, v); }
DEVI float u2f(unsigned v) { return __builtin_bit_cast(float, v); }
struct tw { cf w, wp; };
DEVI cf cmul(cf a, const tw& t) { return FMA2(a.yy, t.wp, a.xx * t.w); }
DEVI cf cmulc(cf a, const tw& t) { return FMA2(a.yy, t.w.yx, a.xx * t.wp.yx); }
DEVI cf addmi(cf a, cf b) { const cf pm = {1.f, -1.f}; return FMA2(b.yx, pm, a); }
DEVI cf addpi(cf a, cf b) { const cf mp = {-1.f, 1.f}; return FMA2(b.yx, mp, a); }
DEVI tw mk_tw(int num, int den, float inv_den, float scale = 1.f) {
  float r = (float)(num % den) * inv_den;
  float c = __builtin_amdgcn_cosf(r) * scale, s = __builtin_amdgcn_sinf(r) * scale;
  tw t; t.w.x = c; t.w.y = -s; t.wp.x = s; t.wp.y = c;
  asm volatile("" : "+v"(t.w), "+v"(t.wp));
  return t;
}
template <bool INV> DEVI cf rotadd(cf a, cf b) { return INV ? addpi(a, b) : addmi(a, b); }
template <bool INV> DEVI cf rotsub(cf a, cf b) { return INV ? addmi(a, b) : addpi(a, b); }
template <bool INV> DEVI void fft8(cf* x) {
  const float h = 0.70710678118654752f;
  const cf hh = {h, h};
  cf a0 = x[0] + x[4], a1 = x[1] + x[5], a2 = x[2] + x[6], a3 = x[3] + x[7];
  cf b0 = x[0] - x[4], b1 = x[1] - x[5], b2 = x[2] - x[6], b3 = x[3] - x[7];
  cf c0 = a0 + a2, c1 = a1 + a3, d0 = a0 - a2, d1 = a1 - a3;
  x[0] = c0 + c1; x[4] = c0 - c1;
  x[2] = rotadd<INV>(d0, d1); x[6] = rotsub<INV>(d0, d1);
  cf e0 = rotadd<INV>(b0, b2), e1 = rotsub<INV>(b0, b2);
  cf p = rotadd<INV>(b1, b3), q = rotsub<INV>(b1, b3);
  cf pp = rotadd<INV>(p, p), qq = rotsub<INV>(q, q);
  x[1] = FMA2(pp, hh, e0); x[5] = FMA2(pp, -hh, e0);
  x[3] = FMA2(qq, -hh, e1); x[7] = FMA2(qq, hh, e1);
}
template <bool INV> DEVI void fft4(cf* x) {
  cf c0 = x[0] + x[2], c1 = x[1] + x[3], d0 = x[0] - x[2], d1 = x[1] - x[3];
  x[0] = c0 + c1; x[2] = c0 - c1;
  x[1] = rotadd<INV>(d0, d1); x[3] = rotsub<INV>(d0, d1);
}
DEVI cf bc(float v) { cf r = {v, v}; return r; }
template <bool INV> DEVI void fft7(cf* x) {
  const float C1 = 0.6234898018587336f, C2 = -0.2225209339563144f, C3 = -0.9009688679024191f;
  const float S1 = 0.7818314824680298f, S2 = 0.9749279121818236f, S3 = 0.4338837391175581f;
  cf t1 = x[1] + x[6], t2 = x[2] + x[5], t3 = x[3] + x[4];
  cf u1 = x[1] - x[6], u2 = x[2] - x[5], u3 = x[3] - x[4];
  cf x0 = x[0];
  cf A1 = FMA2(t3, bc(C3), FMA2(t2, bc(C2), FMA2(t1, bc(C1), x0)));
  cf A2 = FMA2(t3, bc(C1), FMA2(t2, bc(C3), FMA2(t1, bc(C2), x0)));
  cf A3 = FMA2(t3, bc(C2), FMA2(t2, bc(C1), FMA2(t1, bc(C3), x0)));
  cf B1 = FMA2(u3, bc(S3), FMA2(u2, bc(S2), u1 * bc(S1)));
  cf B2 = FMA2(u3, bc(-S1), FMA2(u2, bc(-S3), u1 * bc(S2)));
  cf B3 = FMA2(u3, bc(S2), FMA2(u2, bc(-S1), u1 * bc(S3)));
  x[0] = x0 + (t1 + (t2 + t3));
  x[1] = rotadd<INV>(A1, B1); x[6] = rotsub<INV>(A1, B1);
  x[2] = rotadd<INV>(A2, B2); x[5] = rotsub<INV>(A2, B2);
  x[3] = rotadd<INV>(A3, B3); x[4] = rotsub<INV>(A3, B3);
}
DEVI unsigned pk_bf16(float lo, float hi) {
  cf v = {lo, hi};
  bf2 r = __builtin_convertvector(v, bf2);
  return __builtin_bit_cast(unsigned, r);
}
DEVI float bf_lo(unsigned u) { return __builtin_bit_cast(float, u << 16); }
DEVI float bf_hi(unsigned u) { return __builtin_bit_cast(float, u & 0xffff0000u); }
DEVI cf unpk(unsigned u) { cf r = {bf_lo(u), bf_hi(u)}; return r; }
DEVI float rbf(float v) { return bf_lo(pk_bf16(v, v)); }
DEVI cf rbf2(cf v) { return unpk(pk_bf16(v.x, v.y)); }

template <int CTRL> DEVI float dpp_add(float v) {
  return v + __builtin_bit_cast(float, __builtin_amdgcn_update_dpp(0, __builtin_bit_cast(int, v), CTRL, 0xf, 0xf, true));
}
DEVI float wave_allsum(float v) {
  v = dpp_add<0xB1>(v);
  v = dpp_add<0x4E>(v);
  v = dpp_add<0x141>(v);
  v = dpp_add<0x140>(v);
  unsigned u = __builtin_bit_cast(unsigned, v);
  auto r = __builtin_amdgcn_permlane16_swap(u, u, false, false);
  v = __builtin_bit_cast(float, (unsigned)r[0]) + __builtin_bit_cast(float, (unsigned)r[1]);
  u = __builtin_bit_cast(unsigned, v);
  auto r2 = __builtin_amdgcn_permlane32_swap(u, u, false, false);
  return __builtin_bit_cast(float, (unsigned)r2[0]) + __builtin_bit_cast(float, (unsigned)r2[1]);
}
DEVI void ln2rows(cf* z) {
  cf piv;
  piv.x = u2f(__builtin_amdgcn_readfirstlane(f2u(z[0].x)));
  piv.y = u2f(__builtin_amdgcn_readfirstlane(f2u(z[0].y)));
  cf s1 = {0.f, 0.f}, s2 = {0.f, 0.f};
#pragma unroll
  for (int i = 0; i < 7; i++) { cf d = z[i] - piv; s1 += d; s2 = FMA2(d, d, s2); }
  s1.x = wave_allsum(s1.x); s1.y = wave_allsum(s1.y);
  s2.x = wave_allsum(s2.x); s2.y = wave_allsum(s2.y);
  const cf invn = bc(1.0f / 448.0f);
  cf md = s1 * invn;
  cf var = FMA2(-md, md, s2 * invn);
  var.x = fmaxf(var.x, 0.f); var.y = fmaxf(var.y, 0.f);
  cf mean = rbf2(piv + md);
  var = rbf2(rbf2(var) + bc(1e-5f));
  cf den; den.x = __builtin_amdgcn_sqrtf(var.x); den.y = __builtin_amdgcn_sqrtf(var.y);
  den = rbf2(den);
  cf rs; rs.x = __builtin_amdgcn_rcpf(den.x); rs.y = __builtin_amdgcn_rcpf(den.y);
#pragma unroll
  for (int i = 0; i < 7; i++) z[i] = (z[i] - mean) * rs;
}
DEVI void swap16(unsigned& a, unsigned& b) { auto r = __builtin_amdgcn_permlane16_swap(a, b, false, false); a = r[0]; b = r[1]; }
DEVI void swap32(unsigned& a, unsigned& b) { auto r = __builtin_amdgcn_permlane32_swap(a, b, false, false); a = r[0]; b = r[1]; }
DEVI void swap16c(cf& a, cf& b) {
  unsigned ax = f2u(a.x), ay = f2u(a.y), bx = f2u(b.x), by = f2u(b.y);
  swap16(ax, bx); swap16(ay, by);
  a = cf{u2f(ax), u2f(ay)}; b = cf{u2f(bx), u2f(by)};
}
DEVI void swap32c(cf& a, cf& b) {
  unsigned ax = f2u(a.x), ay = f2u(a.y), bx = f2u(b.x), by = f2u(b.y);
  swap32(ax, bx); swap32(ay, by);
  a = cf{u2f(ax), u2f(ay)}; b = cf{u2f(bx), u2f(by)};
}
DEVI rsrc_t mk_rsrc(const void* p, int bytes) { return __builtin_amdgcn_make_buffer_rsrc((void*)p, 0, bytes, 0x00020000); }
DEVI f32x4 mfma16(v4u a, v4u b, f32x4 c) {
  return __builtin_amdgcn_mfma_f32_16x16x32_bf16(__builtin_bit_cast(bf16x8, a), __builtin_bit_cast(bf16x8, b), c, 0, 0, 0);
}
DEVI int xcd_swz(int b, int per) { return (b & 7) * per + (b >> 3); }

__global__ void __launch_bounds__(256) k0(const unsigned short* __restrict__ wr, const unsigned short* __restrict__ wi,
                                          const unsigned short* __restrict__ c, const unsigned short* __restrict__ wmod,
                                          unsigned char* __restrict__ ws) {
  __shared__ float red[4][32];
  const int tid = threadIdx.x, b = blockIdx.x;
  if (b < 256) {
    const int t = xcd_swz(b, 32);
    const int lane = t & 63, wv = t >> 6;
    const int k1 = lane & 7, k2 = ((lane >> 3) & 1) + 2 * wv;
    const int base = k1 + 8 * k2 + 64 * (lane >> 4);
    if (tid < KD) {
      unsigned o[8];
#pragma unroll
      for (int r = 0; r < 8; r++) {
        const int ks = base + 256 * (r >> 2) + 512 * (r & 3);
        o[r] = (unsigned)wr[ks * KD + tid] | ((unsigned)wi[ks * KD + tid] << 16);
      }
      v4u* dst = (v4u*)(ws + OFF_WT + tid * 8192 + t * 32);
      dst[0] = v4u{o[0], o[1], o[2], o[3]};
      dst[1] = v4u{o[4], o[5], o[6], o[7]};
    }
  } else {
    const int cb = b - 256;
    const int ch = tid & 3, r0 = tid >> 2;
    float acc[8];
#pragma unroll
    for (int j = 0; j < 8; j++) acc[j] = 0.f;
#pragma unroll
    for (int i = 0; i < 7; i++) {
      const int row = r0 + 64 * i;
      const v4u w = *(const v4u*)(wmod + row * 2688 + cb * 32 + ch * 8);
      const float cv = bf_lo(c[row]);
      const float sv = rbf(cv / (1.0f + __expf(-cv)));
#pragma unroll
      for (int j = 0; j < 4; j++) { acc[2 * j] += sv * bf_lo(w[j]); acc[2 * j + 1] += sv * bf_hi(w[j]); }
    }
#pragma unroll
    for (int j = 0; j < 8; j++) {
      float v = acc[j];
      v += __shfl_xor(v, 4); v += __shfl_xor(v, 8); v += __shfl_xor(v, 16); v += __shfl_xor(v, 32);
      if ((tid & 63) < 4) red[tid >> 6][ch * 8 + j] = v;
    }
    __syncthreads();
    if (tid < 32) {
      float v = rbf((red[0][tid] + red[1][tid]) + (red[2][tid] + red[3][tid]));
      const int col = cb * 32 + tid, sec = col / 448;
      if (sec == 1 || sec == 4) v = rbf(1.0f + v);
      ((float*)(ws + OFF_MOD))[col] = v;
    }
  }
}

constexpr int P1_ZS = 504;
__global__ void __launch_bounds__(256) kp1(const unsigned short* __restrict__ x, unsigned char* __restrict__ ws) {
  __shared__ __attribute__((aligned(16))) cf lds[4 * 512 + 4 * 448];
  const int tid = threadIdx.x, lane = tid & 63, wv = __builtin_amdgcn_readfirstlane(tid >> 6);
  const int m = xcd_swz(blockIdx.x, 32);
  const float* mod = (const float*)(ws + OFF_MOD);
  cf z[8];
  float sa[7], a1[7];
  const unsigned short* xa = x + (m + 512 * wv) * D_;
  const unsigned short* xb = xa + 256 * D_;
#pragma unroll
  for (int i = 0; i < 7; i++) { z[i].x = bf_lo(xa[64 * i + lane]); z[i].y = bf_lo(xb[64 * i + lane]); }
#pragma unroll
  for (int i = 0; i < 7; i++) { sa[i] = mod[64 * i + lane]; a1[i] = mod[448 + 64 * i + lane]; }
  tw ta[7], tb[8], tc[8];
  const int l56 = lane < 56 ? lane : 55;
  const int q1 = l56 >> 3, q0 = l56 & 7;
#pragma unroll
  for (int i = 1; i < 7; i++) ta[i] = mk_tw(lane * i, 448, 1.0f / 448.0f);
#pragma unroll
  for (int i = 1; i < 8; i++) tb[i] = mk_tw(q0 * i, 64, 1.0f / 64.0f);
#pragma unroll
  for (int i = 1; i < 8; i++) tc[i] = mk_tw(m * i, 2048, 1.0f / 2048.0f);
  ln2rows(z);
#pragma unroll
  for (int i = 0; i < 7; i++) z[i] = FMA2(z[i], bc(a1[i]), bc(sa[i]));
  fft7<false>(z);
  cf* sc = lds + wv * 512;
  sc[lane] = z[0];
#pragma unroll
  for (int i = 1; i < 7; i++) sc[i * 72 + lane] = cmul(z[i], ta[i]);
  __builtin_amdgcn_fence(__ATOMIC_RELEASE, "wavefront");
  __builtin_amdgcn_wave_barrier();
#pragma unroll
  for (int i = 0; i < 8; i++) z[i] = sc[q1 * 72 + 8 * i + q0];
  __builtin_amdgcn_wave_barrier();
  fft8<false>(z);
#pragma unroll
  for (int i = 0; i < 8; i++) {
    const int f = (2 * q1 + (i >> 2)) & 3;
    cf v = i ? cmul(z[i], tb[i]) : z[0];
    sc[(8 * q1 + i) * 8 + ((((q0 >> 1) ^ f) << 1) | (q0 & 1))] = v;
  }
  __builtin_amdgcn_fence(__ATOMIC_RELEASE, "wavefront");
  __builtin_amdgcn_wave_barrier();
  {
    const int f = (l56 >> 2) & 3;
#pragma unroll
    for (int c = 0; c < 4; c++) {
      f32x4 v = *(const f32x4*)(sc + l56 * 8 + ((c ^ f) << 1));
      z[2 * c].x = v[0]; z[2 * c].y = v[1]; z[2 * c + 1].x = v[2]; z[2 * c + 1].y = v[3];
    }
  }
  fft8<false>(z);
  cf* zs = lds + 4 * 512;
  if (lane < 56) {
#pragma unroll
    for (int i = 0; i < 8; i++) zs[wv * 448 + q1 + 7 * q0 + 56 * i] = z[i];
  }
  __syncthreads();
  if (tid < KD) {
    const int kn = tid ? 448 - tid : 0;
    const cf pm = {1.f, -1.f};
#pragma unroll
    for (int j = 0; j < 4; j++) {
      cf zk = zs[j * 448 + tid], zn = zs[j * 448 + kn];
      z[2 * j] = FMA2(zn, pm, zk);
      z[2 * j + 1] = FMA2(zk.yx, pm, zn.yx);
    }
    fft8<false>(z);
    unsigned o[8];
    o[0] = pk_bf16(z[0].x, z[0].y);
#pragma unroll
    for (int i = 1; i < 8; i++) { cf v = cmul(z[i], tc[i]); o[i] = pk_bf16(v.x, v.y); }
    v4u* dst = (v4u*)(ws + OFF_Z1 + tid * 8192 + m * 32);
    dst[0] = v4u{o[0], o[1], o[2], o[3]};
    dst[1] = v4u{o[4], o[5], o[6], o[7]};
  }
}

__global__ void __launch_bounds__(256) kp2(unsigned char* __restrict__ ws) {
  __shared__ __attribute__((aligned(16))) cf lds[2048];
  const int tid = threadIdx.x, lane = tid & 63, wv = __builtin_amdgcn_readfirstlane(tid >> 6);
  const int k = blockIdx.x;
  const int kb = lane & 1, m2 = ((lane >> 1) & 7) + 8 * wv, kap = lane >> 4;
  const int goff = k * 8192 + (32 * kap + m2) * 32 + kb * 16;
  v4u in0 = *(const v4u*)(ws + OFF_Z1 + goff), in1 = *(const v4u*)(ws + OFF_Z1 + goff + 4096);
  v4u w0 = *(const v4u*)(ws + OFF_WT + k * 8192 + tid * 32), w1 = *(const v4u*)(ws + OFF_WT + k * 8192 + tid * 32 + 16);
  tw ta[8], tb[8], W[8];
#pragma unroll
  for (int i = 1; i < 8; i++) ta[i] = mk_tw(m2 * i, 256, 1.0f / 256.0f);
#pragma unroll
  for (int i = 1; i < 8; i++) tb[i] = mk_tw(kap * i, 32, 1.0f / 32.0f);
  int ea, eb0;
  {
    const int m3 = m2 & 3, n3 = m2 >> 2;
    ea = (m3 & 1) | (kb << 1) | (((kap & 1) ^ (m3 >> 1)) << 2) | (((kap >> 1) ^ (n3 & 1)) << 3) | ((m3 >> 1) << 5) | (n3 << 6);
    const int k1 = lane & 7, k2b0 = (lane >> 3) & 1;
    eb0 = (kap & 1) | ((k1 >> 2) << 1) | (((k1 & 1) ^ (kap >> 1)) << 2) | (((k1 >> 1) & 1) << 3) | (k2b0 << 4) | ((kap >> 1) << 5) | (wv << 9);
  }
  unsigned u[8] = {in0[0], in0[1], in0[2], in0[3], in1[0], in1[1], in1[2], in1[3]};
#pragma unroll
  for (int h = 0; h < 2; h++) {
    swap16(u[4 * h], u[4 * h + 1]); swap16(u[4 * h + 2], u[4 * h + 3]);
    swap32(u[4 * h], u[4 * h + 2]); swap32(u[4 * h + 1], u[4 * h + 3]);
  }
  cf z[8];
#pragma unroll
  for (int i = 0; i < 8; i++) z[i] = unpk(u[i]);
#pragma unroll
  for (int i = 0; i < 8; i++) {
    const unsigned wu = i < 4 ? w0[i] : w1[i - 4];
    W[i].w = unpk(wu); W[i].wp.x = -W[i].w.y; W[i].wp.y = W[i].w.x;
  }
  fft8<false>(z);
#pragma unroll
  for (int i = 0; i < 8; i++) lds[ea + ((i & 1) << 4) + ((i >> 1) << 9)] = i ? cmul(z[i], ta[i]) : z[0];
  __syncthreads();
#pragma unroll
  for (int i = 0; i < 8; i++) z[i] = lds[(eb0 ^ ((i & 1) << 3)) + (i << 6)];
  fft8<false>(z);
#pragma unroll
  for (int i = 1; i < 8; i++) z[i] = cmul(z[i], tb[i]);
#pragma unroll
  for (int h = 0; h < 2; h++) {
    swap16c(z[4 * h], z[4 * h + 1]); swap16c(z[4 * h + 2], z[4 * h + 3]);
    swap32c(z[4 * h], z[4 * h + 2]); swap32c(z[4 * h + 1], z[4 * h + 3]);
  }
  fft4<false>(z); fft4<false>(z + 4);
#pragma unroll
  for (int i = 0; i < 8; i++) z[i] = cmul(z[i], W[i]);
  fft4<true>(z); fft4<true>(z + 4);
#pragma unroll
  for (int h = 0; h < 2; h++) {
    swap16c(z[4 * h], z[4 * h + 1]); swap16c(z[4 * h + 2], z[4 * h + 3]);
    swap32c(z[4 * h], z[4 * h + 2]); swap32c(z[4 * h + 1], z[4 * h + 3]);
  }
#pragma unroll
  for (int i = 1; i < 8; i++) z[i] = cmulc(z[i], tb[i]);
  fft8<true>(z);
#pragma unroll
  for (int i = 0; i < 8; i++) lds[(eb0 ^ ((i & 1) << 3)) + (i << 6)] = z[i];
  __syncthreads();
#pragma unroll
  for (int i = 0; i < 8; i++) { cf v = lds[ea + ((i & 1) << 4) + ((i >> 1) << 9)]; z[i] = i ? cmulc(v, ta[i]) : v; }
  fft8<true>(z);
#pragma unroll
  for (int i = 0; i < 8; i++) u[i] = pk_bf16(z[i].x, z[i].y);
#pragma unroll
  for (int h = 0; h < 2; h++) {
    swap16(u[4 * h], u[4 * h + 1]); swap16(u[4 * h + 2], u[4 * h + 3]);
    swap32(u[4 * h], u[4 * h + 2]); swap32(u[4 * h + 1], u[4 * h + 3]);
  }
  *(v4u*)(ws + OFF_Z2 + goff) = v4u{u[0], u[1], u[2], u[3]};
  *(v4u*)(ws + OFF_Z2 + goff + 4096) = v4u{u[4], u[5], u[6], u[7]};
}

__global__ void __launch_bounds__(256) kp3(const unsigned short* __restrict__ x, unsigned char* __restrict__ ws) {
  __shared__ __attribute__((aligned(16))) cf lds[4 * 512 + 4 * 448];
  __shared__ __attribute__((aligned(16))) unsigned short stg[2][8][456];
  const int tid = threadIdx.x, lane = tid & 63, wv = __builtin_amdgcn_readfirstlane(tid >> 6);
  const int m = xcd_swz(blockIdx.x, 32);
  const float* mod = (const float*)(ws + OFF_MOD);
  const int kk = tid < KD ? tid : KD - 1;
  const v4u in0 = *(const v4u*)(ws + OFF_Z2 + kk * 8192 + m * 32), in1 = *(const v4u*)(ws + OFF_Z2 + kk * 8192 + m * 32 + 16);
  cf xr[7];
  float ga[7], sb[7], a2[7];
  const unsigned short* xa = x + (m + 512 * wv) * D_;
  const unsigned short* xb = xa + 256 * D_;
#pragma unroll
  for (int i = 0; i < 7; i++) { xr[i].x = bf_lo(xa[64 * i + lane]); xr[i].y = bf_lo(xb[64 * i + lane]); }
#pragma unroll
  for (int i = 0; i < 7; i++) { ga[i] = mod[2 * 448 + 64 * i + lane]; sb[i] = mod[3 * 448 + 64 * i + lane]; a2[i] = mod[4 * 448 + 64 * i + lane]; }
  tw ta[7], tb[8], tc[8];
  const int l56 = lane < 56 ? lane : 55;
  const int q1 = l56 >> 3, q0 = l56 & 7;
  const float scale = 1.0f / (2.0f * 2048.0f * 448.0f);
#pragma unroll
  for (int i = 1; i < 7; i++) ta[i] = mk_tw(lane * i, 448, 1.0f / 448.0f);
#pragma unroll
  for (int i = 1; i < 8; i++) tb[i] = mk_tw(q0 * i, 64, 1.0f / 64.0f);
#pragma unroll
  for (int i = 1; i < 8; i++) tc[i] = mk_tw(m * i, 2048, 1.0f / 2048.0f, scale);
  cf z[8];
  cf* zs = lds + 4 * 512;
  {
    z[0] = unpk(in0[0]) * bc(scale);
#pragma unroll
    for (int i = 1; i < 8; i++) z[i] = cmulc(unpk(i < 4 ? in0[i] : in1[i - 4]), tc[i]);
    fft8<true>(z);
    if (tid == 0 || tid == 224) {
#pragma unroll
      for (int i = 0; i < 8; i++) z[i].y = 0.f;
    }
    const int kn = tid ? 448 - tid : 0;
    const cf pm = {1.f, -1.f};
    if (tid < KD) {
#pragma unroll
      for (int j = 0; j < 4; j++) {
        zs[j * 448 + tid] = addpi(z[2 * j], z[2 * j + 1]);
        zs[j * 448 + kn] = FMA2(z[2 * j], pm, z[2 * j + 1].yx);
      }
    }
  }
  __syncthreads();
  cf* sc = lds + wv * 512;
#pragma unroll
  for (int i = 0; i < 8; i++) z[i] = zs[wv * 448 + q1 + 7 * q0 + 56 * i];
  fft8<true>(z);
#pragma unroll
  for (int i = 1; i < 8; i++) z[i] = cmulc(z[i], tb[i]);
  {
    const int f = (l56 >> 2) & 3;
#pragma unroll
    for (int c = 0; c < 4; c++) {
      f32x4 v = {z[2 * c].x, z[2 * c].y, z[2 * c + 1].x, z[2 * c + 1].y};
      *(f32x4*)(sc + l56 * 8 + ((c ^ f) << 1)) = v;
    }
  }
  __builtin_amdgcn_fence(__ATOMIC_RELEASE, "wavefront");
  __builtin_amdgcn_wave_barrier();
#pragma unroll
  for (int i = 0; i < 8; i++) {
    const int f = (2 * q1 + (i >> 2)) & 3;
    z[i] = sc[(8 * q1 + i) * 8 + ((((q0 >> 1) ^ f) << 1) | (q0 & 1))];
  }
  __builtin_amdgcn_wave_barrier();
  fft8<true>(z);
#pragma unroll
  for (int i = 0; i < 8; i++) sc[q1 * 72 + 8 * i + q0] = z[i];
  __builtin_amdgcn_fence(__ATOMIC_RELEASE, "wavefront");
  __builtin_amdgcn_wave_barrier();
  z[0] = sc[lane];
#pragma unroll
  for (int i = 1; i < 7; i++) z[i] = cmulc(sc[i * 72 + lane], ta[i]);
  fft7<true>(z);
#pragma unroll
  for (int i = 0; i < 7; i++) {
    const cf y = rbf2(z[i]);
    const cf t = rbf2(y * bc(ga[i]));
    const unsigned u = pk_bf16(xr[i].x + t.x, xr[i].y + t.y);
    stg[0][2 * wv][64 * i + lane] = (unsigned short)u;
    stg[0][2 * wv + 1][64 * i + lane] = (unsigned short)(u >> 16);
    z[i] = unpk(u);
  }
  ln2rows(z);
#pragma unroll
  for (int i = 0; i < 7; i++) {
    const cf h = FMA2(z[i], bc(a2[i]), bc(sb[i]));
    const unsigned u = pk_bf16(h.x, h.y);
    stg[1][2 * wv][64 * i + lane] = (unsigned short)u;
    stg[1][2 * wv + 1][64 * i + lane] = (unsigned short)(u >> 16);
  }
  __syncthreads();
#pragma unroll
  for (int r = 0; r < 2; r++) {
    const int e = tid + 256 * r;
    if (e < 448) {
      {
        const int row = e / 56, ch = e % 56;
        *(v4u*)(ws + OFF_X1 + (8 * m + row) * 896 + ch * 16) = *(const v4u*)(&stg[0][row][ch * 8]);
      }
      {
        const int row = e & 7, oc = e >> 3;
        *(v4u*)(ws + OFF_H2 + (m >> 1) * 14336 + oc * 256 + ((m & 1) * 8 + row) * 16) = *(const v4u*)(&stg[1][row][oc * 8]);
      }
    }
  }
}

constexpr int W1_FR = 1040;
__global__ void __launch_bounds__(256, 1) kg1(const unsigned short* __restrict__ wg, const unsigned short* __restrict__ wu,
                                              unsigned char* __restrict__ ws) {
  extern __shared__ __attribute__((aligned(16))) unsigned char smem[];
  const int tid = threadIdx.x, lane = tid & 63, wv = __builtin_amdgcn_readfirstlane(tid >> 6);
  const int b = blockIdx.x;
  const int q = (b & 7) * 4 + ((b >> 3) & 3), p = b >> 5;
  v4u bf[4][14];
  const unsigned char* h2 = ws + OFF_H2 + (16 * p + 4 * wv) * 14336 + lane * 16;
#pragma unroll
  for (int ks = 0; ks < 14; ks++)
#pragma unroll
    for (int tt = 0; tt < 4; tt++) bf[tt][ks] = *(const v4u*)(h2 + tt * 14336 + ks * 1024);
#pragma unroll
  for (int r = 0; r < 4; r++) {
    const int it = tid + 256 * r;
    if (it < 784) {
      const int T = it % 7, rest = it / 7, ko = rest % 56, ty = rest / 56;
      const unsigned short* src = (ty ? wu : wg) + (8 * ko) * F_ + 56 * q + 8 * T;
      v4u rr[8];
#pragma unroll
      for (int j = 0; j < 8; j++) rr[j] = *(const v4u*)(src + j * F_);
      unsigned char* dst = smem + ((ko >> 2) * 7 + T) * W1_FR + ((ko & 3) * 16 + 2 * ty) * 16;
#pragma unroll
      for (int i = 0; i < 8; i++) {
        const unsigned sel = (i & 1) ? 0x07060302u : 0x05040100u;
        v4u o;
#pragma unroll
        for (int jj = 0; jj < 4; jj++) o[jj] = __builtin_amdgcn_perm(rr[2 * jj + 1][i >> 1], rr[2 * jj][i >> 1], sel);
        *(v4u*)(dst + (4 * (i >> 1) + (i & 1)) * 16) = o;
      }
    }
  }
  __syncthreads();
  f32x4 acc[7][4];
#pragma unroll
  for (int T = 0; T < 7; T++)
#pragma unroll
    for (int tt = 0; tt < 4; tt++) acc[T][tt] = f32x4{0.f, 0.f, 0.f, 0.f};
#pragma unroll
  for (int ks = 0; ks < 14; ks++)
#pragma unroll
    for (int T = 0; T < 7; T++) {
      const v4u a = *(const v4u*)(smem + (ks * 7 + T) * W1_FR + lane * 16);
#pragma unroll
      for (int tt = 0; tt < 4; tt++) acc[T][tt] = mfma16(a, bf[tt][ks], acc[T][tt]);
    }
  unsigned char* hd = ws + OFF_H + (16 * p + 4 * wv + (lane >> 4)) * (224 * 256) + 7 * q * 256 + (lane & 15) * 16;
#pragma unroll
  for (int T = 0; T < 7; T++) {
    unsigned d[4];
#pragma unroll
    for (int tt = 0; tt < 4; tt++) {
      const cf g = rbf2(cf{acc[T][tt][0], acc[T][tt][1]}), u = rbf2(cf{acc[T][tt][2], acc[T][tt][3]});
      d[tt] = pk_bf16(fmaxf(g.x, 0.f) * u.x, fmaxf(g.y, 0.f) * u.y);
    }
    swap16(d[0], d[1]); swap16(d[2], d[3]);
    swap32(d[0], d[2]); swap32(d[1], d[3]);
    *(v4u*)(hd + T * 256) = v4u{d[0], d[1], d[2], d[3]};
  }
}

__global__ void __launch_bounds__(256, 1) kg2(const unsigned short* __restrict__ wd, unsigned char* __restrict__ ws,
                                              unsigned short* __restrict__ out) {
  __shared__ __attribute__((aligned(16))) f32x4 red[4][4][4][64];
  const int tid = threadIdx.x, lane = tid & 63, wv = __builtin_amdgcn_readfirstlane(tid >> 6);
  const int b = blockIdx.x;
  const int xc = b & 7, ix = b >> 3;
  const int p = xc, sub = ix / 7, ct = ix % 7;
  const int n0 = ct * 64;
  const int c = lane & 15, g = lane >> 4;
  int tile[4] = {16 * p + 2 * sub, 16 * p + 2 * sub + 1, 16 * p + 8 + 2 * sub, 16 * p + 8 + 2 * sub + 1};
  f32x4 acc[4][4];
#pragma unroll
  for (int i = 0; i < 4; i++)
#pragma unroll
    for (int tt = 0; tt < 4; tt++) acc[i][tt] = f32x4{0.f, 0.f, 0.f, 0.f};
  const unsigned short* wsrc = wd + (448 * wv + 8 * g) * D_ + n0 + 4 * c;
  const unsigned char* hsrc = ws + OFF_H + (14 * wv) * 1024 + lane * 16;
#pragma unroll
  for (int ks = 0; ks < 14; ks++) {
    v2u rr[8];
#pragma unroll
    for (int j = 0; j < 8; j++) rr[j] = *(const v2u*)(wsrc + (32 * ks + j) * D_);
    v4u hb[4];
#pragma unroll
    for (int tt = 0; tt < 4; tt++) hb[tt] = *(const v4u*)(hsrc + tile[tt] * (224 * 256) + ks * 1024);
#pragma unroll
    for (int i = 0; i < 4; i++) {
      const unsigned sel = (i & 1) ? 0x07060302u : 0x05040100u;
      v4u a;
#pragma unroll
      for (int jj = 0; jj < 4; jj++) a[jj] = __builtin_amdgcn_perm(rr[2 * jj + 1][i >> 1], rr[2 * jj][i >> 1], sel);
#pragma unroll
      for (int tt = 0; tt < 4; tt++) acc[i][tt] = mfma16(a, hb[tt], acc[i][tt]);
    }
  }
#pragma unroll
  for (int tt = 0; tt < 4; tt++)
#pragma unroll
    for (int i = 0; i < 4; i++) red[wv][tt][i][lane] = acc[i][tt];
  __syncthreads();
  const int vt = (wv < 2 ? 16 * p + 2 * sub + wv : 16 * p + 8 + 2 * sub + wv - 2) * 16 + c;
  const int srow = (vt >> 3) + 256 * (vt & 7);
  const float* gb = (const float*)(ws + OFF_MOD) + 5 * 448 + n0 + 16 * g;
  const v4u* x1p = (const v4u*)(ws + OFF_X1 + vt * 896 + (n0 + 16 * g) * 2);
  const v4u xa = x1p[0], xb = x1p[1];
  float o[16];
#pragma unroll
  for (int i = 0; i < 4; i++) {
    f32x4 s = (red[0][wv][i][lane] + red[1][wv][i][lane]) + (red[2][wv][i][lane] + red[3][wv][i][lane]);
#pragma unroll
    for (int r = 0; r < 4; r++) o[4 * r + i] = s[r];
  }
  unsigned ou[8];
#pragma unroll
  for (int e = 0; e < 8; e++) {
    const cf y = rbf2(cf{o[2 * e], o[2 * e + 1]});
    const cf t = rbf2(y * cf{gb[2 * e], gb[2 * e + 1]});
    const cf xv = unpk(e < 4 ? xa[e] : xb[e - 4]);
    ou[e] = pk_bf16(xv.x + t.x, xv.y + t.y);
  }
  v4u* op = (v4u*)(out + srow * D_ + n0 + 16 * g);
  op[0] = v4u{ou[0], ou[1], ou[2], ou[3]};
  op[1] = v4u{ou[4], ou[5], ou[6], ou[7]};
}

extern "C" void kernel_launch(void* const* d_in, const int* in_sizes, int n_in, void* d_out, int out_size, void* d_ws,
                              size_t ws_size, hipStream_t stream) {
  const unsigned short* x = (const unsigned short*)d_in[0];
  const unsigned short* wr = (const unsigned short*)d_in[1];
  const unsigned short* wi = (const unsigned short*)d_in[2];
  const unsigned short* wg = (const unsigned short*)d_in[4];
  const unsigned short* wu = (const unsigned short*)d_in[5];
  const unsigned short* wd = (const unsigned short*)d_in[6];
  const unsigned short* c = (const unsigned short*)d_in[7];
  const unsigned short* wmod = (const unsigned short*)d_in[8];
  unsigned char* ws = (unsigned char*)d_ws;
  (void)hipFuncSetAttribute((const void*)kg1, hipFuncAttributeMaxDynamicSharedMemorySize, 98 * W1_FR);
  k0<<<256 + 84, 256, 0, stream>>>(wr, wi, c, wmod, ws);
  kp1<<<256, 256, 0, stream>>>(x, ws);
  kp2<<<KD, 256, 0, stream>>>(ws);
  kp3<<<256, 256, 0, stream>>>(x, ws);
  kg1<<<256, 256, 98 * W1_FR, stream>>>(wg, wu, ws);
  kg2<<<224, 256, 0, stream>>>(wd, ws, (unsigned short*)d_out);
}
```

```cpp
#include <hip/hip_runtime.h>
#include <hip/hip_bf16.h>

typedef float cf __attribute__((ext_vector_type(2)));
typedef float f32x4 __attribute__((ext_vector_type(4)));
typedef unsigned v4u __attribute__((ext_vector_type(4)));
typedef unsigned v2u __attribute__((ext_vector_type(2)));
typedef __bf16 bf2 __attribute__((ext_vector_type(2)));
typedef __amdgpu_buffer_rsrc_t rsrc_t;
#define FMA2 __builtin_elementwise_fma
#define DEVI __device__ __forceinline__
#define RARE(c) __builtin_expect(!!(c), 0)

constexpr int S_ = 2048, D_ = 448, F_ = 1792, KD = 225;
constexpr int OFF_E = 0;
constexpr int OFF_FP = 2048;
constexpr int OFF_FQ = 4096;
constexpr int OFF_FR = 6144;
constexpr int MG_REP = 8;
constexpr int OFF_MG = 8192;
constexpr int OFF_M2 = OFF_MG + MG_REP * 28672;
constexpr int OFF_FD = OFF_M2 + 8192;
constexpr int OFF_FE = OFF_FD + 256 * 128;
constexpr int ZST = 7296;
constexpr int OFF_WT = OFF_FE + 2048 * 128;
constexpr int OFF_Z1 = OFF_WT + 256 * ZST;
constexpr int OFF_Z2 = OFF_Z1 + 256 * ZST;
constexpr int OFF_X1 = OFF_Z2 + 256 * ZST;
constexpr int OFF_H2 = OFF_X1 + S_ * D_ * 2;
constexpr int OFF_H = OFF_H2 + S_ * D_ * 2;
constexpr int OFF_W2P = OFF_H + S_ * F_ * 2;
constexpr int WS_BYTES = OFF_W2P + D_ * F_ * 2;
constexpr int CODE_BYTES = 39936;
constexpr int SC1 = 16;
constexpr int OOB = 0x40000000;
constexpr unsigned SPIN_CAP = 1u << 18;

DEVI unsigned f2u(float v) { return __builtin_bit_cast(unsigned, v); }
DEVI float u2f(unsigned v) { return __builtin_bit_cast(float, v); }
typedef cf tw;
DEVI cf cmul(cf a, cf w) {
  cf t;
  asm("v_pk_mul_f32 %0, %1, %2 op_sel:[1,1] op_sel_hi:[1,0] neg_lo:[1,0]" : "=v"(t) : "v"(a), "v"(w));
  return FMA2(a.xx, w, t);
}
DEVI cf cmulc(cf a, cf w) {
  cf t;
  asm("v_pk_mul_f32 %0, %1, %2 op_sel_hi:[0,1] neg_hi:[0,1]" : "=v"(t) : "v"(a), "v"(w));
  return FMA2(a.yy, w.yx, t);
}
DEVI cf addmi(cf a, cf b) { const cf pm = {1.f, -1.f}; return FMA2(b.yx, pm, a); }
DEVI cf addpi(cf a, cf b) { const cf mp = {-1.f, 1.f}; return FMA2(b.yx, mp, a); }
template <int N> DEVI void mk_pow(tw* t, int num, float inv_den) {
  const float r = (float)num * inv_den;
  const float c = __builtin_amdgcn_cosf(r), s = __builtin_amdgcn_sinf(r);
  t[1] = cf{c, -s};
  asm volatile("s_nop 0" : "+v"(t[1]));
#pragma unroll
  for (int i = 2; i < N; i++) t[i] = cmul(t[i - (i >> 1)], t[i >> 1]);
#pragma unroll
  for (int i = 1; i < N; i++) asm volatile("" : "+v"(t[i]));
}
template <bool INV> DEVI cf rotadd(cf a, cf b) { return INV ? addpi(a, b) : addmi(a, b); }
template <bool INV> DEVI cf rotsub(cf a, cf b) { return INV ? addmi(a, b) : addpi(a, b); }
template <bool INV> DEVI void fft8(cf* x) {
  const float h = 0.70710678118654752f;
  const cf hh = {h, h};
  cf a0 = x[0] + x[4], a1 = x[1] + x[5], a2 = x[2] + x[6], a3 = x[3] + x[7];
  cf b0 = x[0] - x[4], b1 = x[1] - x[5], b2 = x[2] - x[6], b3 = x[3] - x[7];
  cf c0 = a0 + a2, c1 = a1 + a3, d0 = a0 - a2, d1 = a1 - a3;
  x[0] = c0 + c1; x[4] = c0 - c1;
  x[2] = rotadd<INV>(d0, d1); x[6] = rotsub<INV>(d0, d1);
  cf e0 = rotadd<INV>(b0, b2), e1 = rotsub<INV>(b0, b2);
  cf p = rotadd<INV>(b1, b3), q = rotsub<INV>(b1, b3);
  cf pp = rotadd<INV>(p, p), qq = rotsub<INV>(q, q);
  x[1] = FMA2(pp, hh, e0); x[5] = FMA2(pp, -hh, e0);
  x[3] = FMA2(qq, -hh, e1); x[7] = FMA2(qq, hh, e1);
}
template <bool INV> DEVI void fft4(cf* x) {
  cf c0 = x[0] + x[2], c1 = x[1] + x[3], d0 = x[0] - x[2], d1 = x[1] - x[3];
  x[0] = c0 + c1; x[2] = c0 - c1;
  x[1] = rotadd<INV>(d0, d1); x[3] = rotsub<INV>(d0, d1);
}
DEVI cf bc(float v) { return cf{v, v}; }
template <bool INV> DEVI void fft7(cf* x) {
  const float C1 = 0.6234898018587336f, C2 = -0.2225209339563144f, C3 = -0.9009688679024191f;
  const float S1 = 0.7818314824680298f, S2 = 0.9749279121818236f, S3 = 0.4338837391175581f;
  cf t1 = x[1] + x[6], t2 = x[2] + x[5], t3 = x[3] + x[4];
  cf u1 = x[1] - x[6], u2 = x[2] - x[5], u3 = x[3] - x[4];
  cf x0 = x[0];
  cf A1 = FMA2(t3, bc(C3), FMA2(t2, bc(C2), FMA2(t1, bc(C1), x0)));
  cf A2 = FMA2(t3, bc(C1), FMA2(t2, bc(C3), FMA2(t1, bc(C2), x0)));
  cf A3 = FMA2(t3, bc(C2), FMA2(t2, bc(C1), FMA2(t1, bc(C3), x0)));
  cf B1 = FMA2(u3, bc(S3), FMA2(u2, bc(S2), u1 * bc(S1)));
  cf B2 = FMA2(u3, bc(-S1), FMA2(u2, bc(-S3), u1 * bc(S2)));
  cf B3 = FMA2(u3, bc(S2), FMA2(u2, bc(-S1), u1 * bc(S3)));
  x[0] = x0 + (t1 + (t2 + t3));
  x[1] = rotadd<INV>(A1, B1); x[6] = rotsub<INV>(A1, B1);
  x[2] = rotadd<INV>(A2, B2); x[5] = rotsub<INV>(A2, B2);
  x[3] = rotadd<INV>(A3, B3); x[4] = rotsub<INV>(A3, B3);
}
DEVI unsigned pk_bf16(float lo, float hi) { return __builtin_bit_cast(unsigned, __builtin_convertvector(cf{lo, hi}, bf2)); }
DEVI float bf_lo(unsigned u) { return u2f(u << 16); }
DEVI float bf_hi(unsigned u) { return u2f(u & 0xffff0000u); }
DEVI cf unpk(unsigned u) { return cf{bf_lo(u), bf_hi(u)}; }
DEVI float rbf(float v) { return bf_lo(pk_bf16(v, v)); }
DEVI cf rbf2(cf v) { return unpk(pk_bf16(v.x, v.y)); }
DEVI void swap16(unsigned& a, unsigned& b) { auto r = __builtin_amdgcn_permlane16_swap(a, b, false, false); a = r[0]; b = r[1]; }
DEVI void swap32(unsigned& a, unsigned& b) { auto r = __builtin_amdgcn_permlane32_swap(a, b, false, false); a = r[0]; b = r[1]; }
DEVI void tr4(unsigned& a, unsigned& b, unsigned& c, unsigned& d) { swap16(a, b); swap16(c, d); swap32(a, c); swap32(b, d); }
DEVI void tr4c(cf* z) {
  unsigned x[4], y[4];
#pragma unroll
  for (int i = 0; i < 4; i++) { x[i] = f2u(z[i].x); y[i] = f2u(z[i].y); }
  tr4(x[0], x[1], x[2], x[3]); tr4(y[0], y[1], y[2], y[3]);
#pragma unroll
  for (int i = 0; i < 4; i++) z[i] = cf{u2f(x[i]), u2f(y[i])};
}
template <int CTRL> DEVI float dpp_add(float v) { return v + u2f(__builtin_amdgcn_update_dpp(0, f2u(v), CTRL, 0xf, 0xf, true)); }
DEVI void wave_allsum4(float* v) {
#pragma unroll
  for (int i = 0; i < 4; i++) v[i] = dpp_add<0xB1>(v[i]);
#pragma unroll
  for (int i = 0; i < 4; i++) v[i] = dpp_add<0x4E>(v[i]);
#pragma unroll
  for (int i = 0; i < 4; i++) v[i] = dpp_add<0x141>(v[i]);
#pragma unroll
  for (int i = 0; i < 4; i++) v[i] = dpp_add<0x140>(v[i]);
  unsigned a = f2u(v[0]), b = f2u(v[1]), c = f2u(v[2]), d = f2u(v[3]);
  tr4(a, b, c, d);
  float s = (u2f(a) + u2f(b)) + (u2f(c) + u2f(d));
#pragma unroll
  for (int i = 0; i < 4; i++) v[i] = u2f(__builtin_amdgcn_readlane(f2u(s), 16 * i));
}
DEVI void ln2rows(cf* z) {
  const cf piv = {u2f(__builtin_amdgcn_readfirstlane(f2u(z[0].x))), u2f(__builtin_amdgcn_readfirstlane(f2u(z[0].y)))};
  cf s1 = {0.f, 0.f}, s2 = {0.f, 0.f};
#pragma unroll
  for (int i = 0; i < 7; i++) { const cf d = z[i] - piv; s1 += d; s2 = FMA2(d, d, s2); }
  float v[4] = {s1.x, s1.y, s2.x, s2.y};
  wave_allsum4(v);
  const cf invn = bc(1.0f / 448.0f);
  const cf md = cf{v[0], v[1]} * invn;
  cf var = FMA2(-md, md, cf{v[2], v[3]} * invn);
  var = cf{fmaxf(var.x, 0.f), fmaxf(var.y, 0.f)};
  const cf mean = rbf2(piv + md);
  var = rbf2(rbf2(var) + bc(1e-5f));
  const cf den = rbf2(cf{__builtin_amdgcn_sqrtf(var.x), __builtin_amdgcn_sqrtf(var.y)});
  const cf rs = {__builtin_amdgcn_rcpf(den.x), __builtin_amdgcn_rcpf(den.y)};
  const cf nm = -mean * rs;
#pragma unroll
  for (int i = 0; i < 7; i++) z[i] = FMA2(z[i], rs, nm);
}
DEVI rsrc_t mk_rsrc(const void* p, int bytes) { return __builtin_amdgcn_make_buffer_rsrc((void*)p, 0, bytes, 0x00020000); }

#define BAR() asm volatile("s_waitcnt lgkmcnt(0)\n\ts_barrier" ::: "memory")
#define DRAIN() asm volatile("s_waitcnt vmcnt(0)" ::: "memory")
#define CFENCE() asm volatile("" ::: "memory")
#define SB() __builtin_amdgcn_sched_barrier(0)
#define PINV(x) asm volatile("" : "+v"(x))
DEVI v4u ldc(rsrc_t r, int off, int so = 0) { return __builtin_amdgcn_raw_buffer_load_b128(r, off, so, SC1); }
DEVI v4u ldp(rsrc_t r, int off, int so = 0) { return __builtin_amdgcn_raw_buffer_load_b128(r, off, so, 0); }
DEVI void stc(v4u v, rsrc_t r, int off, int so = 0) { __builtin_amdgcn_raw_buffer_store_b128(v, r, off + so, 0, SC1); }
DEVI unsigned umax3(unsigned a, unsigned b, unsigned c) { return max(max(a, b), c); }
DEVI bool poisoned(v4u a, v4u b) {
  return __builtin_amdgcn_ballot_w64(umax3(umax3(a[0], a[1], a[2]), umax3(a[3], b[0], b[1]), max(b[2], b[3])) == 0xffffffffu) != 0;
}
DEVI void repoll2(rsrc_t r, int o0, int o1, v4u& a, v4u& b) {
  if (RARE(poisoned(a, b))) {
    unsigned sp = 0;
#pragma unroll 1
    do { CFENCE(); a = ldc(r, o0); b = ldc(r, o1); } while (poisoned(a, b) && ++sp < SPIN_CAP);
  }
  CFENCE();
}
DEVI v2u ldflag(rsrc_t r, int off) { return __builtin_amdgcn_raw_buffer_load_b64(r, off, 0, SC1); }
DEVI void stflag(rsrc_t r, int off, unsigned tag) { __builtin_amdgcn_raw_buffer_store_b64(v2u{tag, ~tag}, r, off, 0, SC1); }
DEVI bool flag_miss(v2u f, unsigned tag) { return __builtin_amdgcn_ballot_w64(f[0] != tag || f[1] != ~tag) != 0; }
struct f256 { v4u a, b; };
DEVI f256 ld256(rsrc_t r, int off, int lane) { return f256{ldc(r, lane * 16, off), ldc(r, lane * 16 + 1024, off)}; }
DEVI bool miss256(const f256& f, unsigned tag) {
  const unsigned e = ((f.a[0] ^ tag) | (f.a[2] ^ tag) | (f.b[0] ^ tag) | (f.b[2] ^ tag)) | ~((f.a[1] ^ tag) & (f.a[3] ^ tag) & (f.b[1] ^ tag) & (f.b[3] ^ tag));
  return __builtin_amdgcn_ballot_w64(e != 0u) != 0;
}
DEVI void poll256(rsrc_t r, int off, int lane, unsigned tag) {
#pragma unroll 1
  for (unsigned sp = 0; sp < SPIN_CAP; ++sp) {
    CFENCE();
    if (!miss256(ld256(r, off, lane), tag)) break;
    __builtin_amdgcn_s_sleep(8);
  }
  CFENCE();
}
DEVI void pollflag(rsrc_t r, int off, unsigned tag) {
  for (unsigned sp = 0; sp < SPIN_CAP; ++sp) {
    CFENCE();
    if (!flag_miss(ldflag(r, off), tag)) break;
  }
  CFENCE();
}
DEVI v4u col8(const v4u* rr, int i) {
  const unsigned sel = (i & 1) ? 0x07060302u : 0x05040100u;
  v4u o;
#pragma unroll
  for (int jj = 0; jj < 4; jj++) o[jj] = __builtin_amdgcn_perm(rr[2 * jj + 1][i >> 1], rr[2 * jj][i >> 1], sel);
  return o;
}
#define MFMA0(d, a, b) asm volatile("v_mfma_f32_16x16x32_bf16 %0, %1, %2, 0" : "=&v"(d) : "v"(a), "v"(b))
#define MFMA(d, a, b) asm volatile("v_mfma_f32_16x16x32_bf16 %0, %1, %2, %0" : "+v"(d) : "v"(a), "v"(b))
#define MFMA0_A(d, a, b) asm volatile("v_mfma_f32_16x16x32_bf16 %0, %1, %2, 0" : "=&v"(d) : "a"(a), "v"(b))
#define MFMA_A(d, a, b) asm volatile("v_mfma_f32_16x16x32_bf16 %0, %1, %2, %0" : "+v"(d) : "a"(a), "v"(b))

DEVI bool any_nan(float a, float b) { return __builtin_amdgcn_ballot_w64(a != a || b != b) != 0; }
DEVI float relu(float v) { return u2f((unsigned)max((int)f2u(v), 0)); }
DEVI unsigned reglu(f32x4 a) { const cf h = cf{relu(a[0]), relu(a[1])} * cf{a[2], a[3]}; return pk_bf16(h.x, h.y); }
DEVI void g1_epi(const f32x4 (*acc)[2], int gi, rsrc_t rw, int hoff, int hoff3) {
  unsigned d0 = reglu(acc[2 * gi][0]), d1 = reglu(acc[2 * gi][1]), d2, d3;
  if (gi < 3) { d2 = reglu(acc[2 * gi + 1][0]); d3 = reglu(acc[2 * gi + 1][1]); } else { d2 = d0; d3 = d1; }
  tr4(d0, d1, d2, d3);
  stc(v4u{d0, d1, d2, d3}, rw, gi == 3 ? hoff3 : hoff + gi * 512);
}

DEVI void gemv8(float* acc, unsigned cu, v4u w) {
  const float c = bf_lo(cu);
  const float sv = rbf(c * __builtin_amdgcn_rcpf(1.0f + __expf(-c)));
#pragma unroll
  for (int j = 0; j < 4; j++) { acc[2 * j] = fmaf(sv, bf_lo(w[j]), acc[2 * j]); acc[2 * j + 1] = fmaf(sv, bf_hi(w[j]), acc[2 * j + 1]); }
}
DEVI float gemv_reduce(float* acc, float* red, int tid) {
#pragma unroll
  for (int j = 0; j < 8; j++) acc[j] = dpp_add<0x128>(acc[j]);
  if ((tid & 15) < 8) {
    *(f32x4*)(red + (tid >> 4) * 64 + (tid & 7) * 8) = f32x4{acc[0], acc[1], acc[2], acc[3]};
    *(f32x4*)(red + (tid >> 4) * 64 + (tid & 7) * 8 + 4) = f32x4{acc[4], acc[5], acc[6], acc[7]};
  }
  BAR();
  float s0 = 0.f, s1 = 0.f;
#pragma unroll
  for (int r = 0; r < 8; r++) { s0 += red[r * 64 + (tid & 63)]; s1 += red[(r + 8) * 64 + (tid & 63)]; }
  return s0 + s1;
}
template <int V> struct ic { static constexpr int v = V; };
template <int I, int N, class F> DEVI void sfor(F&& f) { if constexpr (I < N) { f(ic<I>{}); sfor<I + 1, N>(f); } }

constexpr int W1_FR = 1040, LDS_SCR = 98 * W1_FR, LDS_M1 = LDS_SCR + 49152, LDS_BYTES = LDS_M1 + 3584;
constexpr int RING = 8;

__global__ void __launch_bounds__(256, 1)
k1(const unsigned short* __restrict__ x, const unsigned short* __restrict__ wr, const unsigned short* __restrict__ wi,
   const unsigned short* __restrict__ wg, const unsigned short* __restrict__ wu, const unsigned short* __restrict__ wd,
   const unsigned short* __restrict__ cvec, const unsigned short* __restrict__ wmod, unsigned char* __restrict__ ws,
   unsigned short* __restrict__ out) {
  extern __shared__ __attribute__((aligned(16))) unsigned char smem[];
  asm volatile("" :: "s"(x), "s"(wr), "s"(wi), "s"(wg), "s"(wu), "s"(wd), "s"(cvec), "s"(wmod), "s"(ws), "s"(out));
  const int tid = threadIdx.x, lane = tid & 63, wv = __builtin_amdgcn_readfirstlane(tid >> 6);
  const int b = blockIdx.x, xc = b & 7, ix = b >> 3;
  const rsrc_t rw = mk_rsrc(ws, WS_BYTES);
  const int m = xc * 32 + ix;
  const bool p2_on = b <= 224;
  const int kc = ix < 28 ? xc * 28 + ix : 224;
  const int q = (xc & 3) * 8 + (ix & 7), p = (xc >> 2) * 4 + (ix >> 3);
  const int ixg = ix < 28 ? ix : 27;
  const int sub = ixg / 7, ct = ixg % 7;
  const int kk = tid < KD ? tid : KD - 1, kn = kk ? 448 - kk : 0;
  const v4u ones = {~0u, ~0u, ~0u, ~0u};

  const unsigned cpf = __builtin_amdgcn_raw_buffer_load_b32(mk_rsrc((const void*)__builtin_amdgcn_s_getpc(), CODE_BYTES), tid < 16 ? (ix * 16 + tid) * 128 : OOB, 0, 0);
  SB();
  const bool mp1 = b < 56;
  const rsrc_t rm = mk_rsrc(wmod, D_ * 2688 * 2), rc = mk_rsrc(cvec, D_ * 2);
  v4u wm[4]; unsigned cv[4];
  const int mr0 = 112 * (b & 3) + (tid >> 3), mo = (mr0 * 2688 + 64 * (b >> 2) + 8 * (tid & 7)) * 2;
#pragma unroll
  for (int i = 0; i < 4; i++) {
    wm[i] = ldp(rm, mp1 && (i < 3 || tid < 128) ? mo : OOB, i * 32 * 2688 * 2);
    cv[i] = __builtin_amdgcn_raw_buffer_load_b16(rc, mp1 && (i < 3 || tid < 128) ? mr0 * 2 : OOB, i * 64, 0);
  }
  const unsigned ep = __builtin_amdgcn_raw_buffer_load_b32(rw, OFF_E, 0, SC1);
  SB();
  stc(ones, rw, tid * 16, OFF_Z1 + b * ZST);
  stc(ones, rw, tid < 200 ? 4096 + tid * 16 : OOB, OFF_Z1 + b * ZST);
  stc(ones, rw, tid * 16, OFF_Z2 + b * ZST);
  stc(ones, rw, tid < 200 ? 4096 + tid * 16 : OOB, OFF_Z2 + b * ZST);
  SB();
  int so_h2[2];
#pragma unroll
  for (int r = 0; r < 2; r++) {
    const int e = tid + 256 * r < 448 ? tid + 256 * r : 447;
    so_h2[r] = OFF_H2 + (m >> 1) * 14336 + (m & 1) * 128 + (e >> 3) * 256 + (e & 7) * 16;
  }
  SB();
  unsigned xu[7];
  {
    const rsrc_t rx = mk_rsrc(x, S_ * D_ * 2);
#pragma unroll
    for (int i = 0; i < 7; i++) xu[i] = __builtin_amdgcn_raw_buffer_load_b32(rx, (lane >> 5) * (256 * D_ * 2) + (lane & 31) * 4 + 128 * i, (m + 512 * wv) * D_ * 2, 0);
  }
  SB();
  const int ml_ = 2 * (lane & 31) + (lane >> 5);
  const int l56 = lane < 56 ? lane : 55;
  const int q1 = l56 >> 3, q0 = l56 & 7;
  const int kb = lane & 1, m2 = ((lane >> 1) & 7) + 8 * wv, kap = lane >> 4;
  tw ta[7], tb[8], tc[8], ua[8], ub[8], W[8];
  mk_pow<7>(ta, ml_, 1.0f / 448.0f);
  mk_pow<8>(tb, q0, 1.0f / 64.0f);
  mk_pow<8>(tc, m, 1.0f / 2048.0f);
  SB();
  cf* const lds = (cf*)(smem + LDS_SCR);
  if (mp1) {
    float acc[8];
#pragma unroll
    for (int j = 0; j < 8; j++) acc[j] = 0.f;
#pragma unroll
    for (int i = 0; i < 4; i++) gemv8(acc, cv[i], wm[i]);
    const float s0 = gemv_reduce(acc, (float*)lds, tid);
#pragma unroll
    for (int r = 0; r < MG_REP / 4; r++)
      __builtin_amdgcn_raw_buffer_store_b64(v2u{f2u(s0), ep + 1u}, rw, lane * 8 + OFF_MG + (wv * (MG_REP / 4) + r) * 28672 + (b & 3) * 7168 + (b >> 2) * 512, 0, SC1);
  }
  SB();
  unsigned fr[8], fi[8];
  const int wpos = ((m >> 3) & 3) * 64 + (m >> 5) * 8 + (m & 7);
  {
    const rsrc_t rr = mk_rsrc(wr, S_ * KD * 2), ri = mk_rsrc(wi, S_ * KD * 2);
    const int l = wpos & 63, base = (l & 7) + 8 * (((l >> 3) & 1) + 2 * (wpos >> 6)) + 64 * (l >> 4);
#pragma unroll
    for (int r = 0; r < 8; r++) {
      const int so = (base + 256 * (r >> 2) + 512 * (r & 3)) * KD * 2;
      fr[r] = __builtin_amdgcn_raw_buffer_load_b16(rr, kk * 2, so, 0); fi[r] = __builtin_amdgcn_raw_buffer_load_b16(ri, kk * 2, so, 0);
    }
  }
  v4u pk[8];
  const int pb = b >= 60 ? b - 60 : 0;
  const rsrc_t rg = mk_rsrc(wg, D_ * F_ * 2), ru = mk_rsrc(wu, D_ * F_ * 2);
  unsigned tch[4];
  {
    const int u = 2 * pb + (lane >> 5);
    const rsrc_t rp = mk_rsrc(wd, wv == 2 && b >= 60 ? D_ * F_ * 2 : 0);
#pragma unroll
    for (int j = 0; j < 8; j++) pk[j] = ldp(rp, ((32 * (u / 7) + 8 * ((lane >> 3) & 3)) * D_ + 64 * (u % 7) + 8 * (lane & 7)) * 2, j * D_ * 2);
    const int to = wv < 2 && lane < 56 && (lane & 7) < 7 ? (14 * ix + 7 * (xc >> 2) + (lane >> 3)) * F_ * 2 + (xc & 3) * 896 + (lane & 7) * 128 : OOB;
    const int mr = 112 * (b & 3) + lane;
    tch[2] = __builtin_amdgcn_raw_buffer_load_b32(rm, wv == 3 && b >= 56 && b < 168 ? (mr * 2688 + 64 * (b >> 2)) * 2 : OOB, 0, 0);
    tch[3] = __builtin_amdgcn_raw_buffer_load_b32(rm, wv == 3 && b >= 56 && b < 168 && lane < 48 ? ((mr + 64) * 2688 + 64 * (b >> 2)) * 2 : OOB, 0, 0);
    tch[0] = tch[1] = __builtin_amdgcn_raw_buffer_load_b32(mk_rsrc(wv ? wu : wg, D_ * F_ * 2), to, 0, 0);
  }
  SB();
  cf z[8], xr[7];
#pragma unroll
  for (int i = 0; i < 7; i++) {
    unsigned a = xu[i] << 16, c = xu[i] & 0xffff0000u;
    swap32(a, c);
    xr[i] = cf{u2f(a), u2f(c)};
    z[i] = xr[i];
  }
  ln2rows(z);
#pragma unroll
  for (int i = 0; i < 7; i++) PINV(z[i]);
  const unsigned tag = __builtin_amdgcn_readfirstlane(ep) + 1u;
  asm volatile("" :: "v"(cpf));
  BAR();
  stflag(rw, tid == 0 ? OFF_FP + b * 8 : OOB, tag);
  int pdst[2];
  cf* const sc = lds + wv * 512;
  cf* const zs = lds + 4 * 512;
  int e1w = (lane >> 5) * 36 + (lane & 31), e1r = q1 * 72 + (q0 & 1) * 36 + (q0 >> 1);
  int zoff = kk * 32 + m * ZST;
  int e2a[2], e2b[4], e3 = wv * 448 + q1 + 7 * q0, kkv = kk, knv = kn;
#pragma unroll
  for (int h = 0; h < 2; h++) { e2a[h] = 64 * q1 + ((((q0 >> 1) ^ ((2 * q1 + h) & 3)) << 1) | (q0 & 1)); PINV(e2a[h]); }
#pragma unroll
  for (int c = 0; c < 4; c++) { e2b[c] = l56 * 8 + ((c ^ ((l56 >> 2) & 3)) << 1); PINV(e2b[c]); }
  PINV(zoff); PINV(e1w); PINV(e1r); PINV(e3); PINV(kkv); PINV(knv);
  SB();
  const int rot1 = (2 * (ix & 7) + 3 * (ix >> 3)) % 14;
  int w1off, w1lds[2];
  cf mp[7];
  v4u wt0, wt1;
  {
    const int go = tid * 16, go2 = tid < 192 ? 4096 + tid * 16 : go, gs = OFF_MG + (b & (MG_REP - 1)) * 28672;
    v4u g[8];
#pragma unroll
    for (int i = 0; i < 4; i++) { g[i] = ldc(rw, go, gs + i * 7168); g[4 + i] = ldc(rw, go2, gs + i * 7168); }
    SB();
#pragma unroll
    for (int r = 0; r < 2; r++) {
      const int u = 2 * pb + r;
      pdst[r] = (b >= 60 ? lane * 16 : OOB) + OFF_W2P + (((u % 7) * 56 + u / 7) * 4 + wv) * 1024;
      PINV(pdst[r]);
    }
  const int l49 = lane < 49 ? lane : 48, sT = l49 % 7, sk = l49 / 7;
  w1off = (8 * (7 * wv + sk) * F_ + 56 * q + 8 * sT) * 2;
#pragma unroll
  for (int h = 0; h < 2; h++) {
    const int u = 7 * (4 * h + wv) + sk, ks = (u >> 2) - rot1;
    w1lds[h] = ((ks < 0 ? ks + 14 : ks) * 7 + sT) * W1_FR + (u & 3) * 256;
  }
    PINV(w1off); PINV(w1lds[0]); PINV(w1lds[1]);
    int m1o[2]; float m1add[2];
#pragma unroll
    for (int h = 0; h < 2; h++) {
      const int col = (h ? go2 : go) >> 3, isc = col >= 448;
      m1o[h] = 2 * (col - 448 * isc) + isc; m1add[h] = isc ? 1.0f : 0.0f;
      PINV(m1o[h]); PINV(m1add[h]);
    }
    wt0 = v4u{fr[0] | fi[0] << 16, fr[1] | fi[1] << 16, fr[2] | fi[2] << 16, fr[3] | fi[3] << 16};
    wt1 = v4u{fr[4] | fi[4] << 16, fr[5] | fi[5] << 16, fr[6] | fi[6] << 16, fr[7] | fi[7] << 16};
    PINV(wt0); PINV(wt1);
    if (wv == 2) {
      unsigned char* d = smem + 4 * (lane >> 5) * 1040 + (((lane >> 3) & 3) * 16 + 2 * (lane & 7)) * 16;
#pragma unroll
      for (int i = 0; i < 8; i++) *(v4u*)(d + (i & 3) * 1040 + (i >> 2) * 16) = col8(pk, i);
    }
    asm volatile("" :: "v"(tch[0]), "v"(tch[1]), "v"(tch[2]), "v"(tch[3]));
    SB();
    auto gbad = [&]() __attribute__((always_inline)) {
      unsigned e = 0;
#pragma unroll
      for (int i = 0; i < 8; i++) e |= (g[i][1] ^ tag) | (g[i][3] ^ tag);
      return __builtin_amdgcn_ballot_w64(e != 0u) != 0;
    };
    if (gbad()) {
      unsigned sp = 0;
#pragma unroll 1
      do {
        CFENCE();
#pragma unroll
        for (int i = 0; i < 4; i++) { g[i] = ldc(rw, go, gs + i * 7168); g[4 + i] = ldc(rw, go2, gs + i * 7168); }
      } while (gbad() && ++sp < SPIN_CAP);
    }
    CFENCE();
    float* mv = (float*)(smem + LDS_M1);
#pragma unroll
    for (int h = 0; h < 2; h++) {
      cf v = rbf2((cf{u2f(g[4 * h][0]), u2f(g[4 * h][2])} + cf{u2f(g[4 * h + 1][0]), u2f(g[4 * h + 1][2])}) +
                  (cf{u2f(g[4 * h + 2][0]), u2f(g[4 * h + 2][2])} + cf{u2f(g[4 * h + 3][0]), u2f(g[4 * h + 3][2])}));
      v = rbf2(v + bc(m1add[h]));
      mv[m1o[h]] = v.x; mv[m1o[h] + 2] = v.y;
    }
    BAR();
#pragma unroll
    for (int i = 0; i < 7; i++) mp[i] = ((const cf*)mv)[64 * i + ml_];
  }
  v4u pko[2];
#pragma unroll
  for (int r = 0; r < 2; r++) pko[r] = *(const v4u*)(smem + (4 * r + wv) * 1040 + lane * 16);
  stc(wt0, rw, kk * 32, OFF_WT + wpos * ZST);
  stc(wt1, rw, kk * 32 + 16, OFF_WT + wpos * ZST);
  const v2u fp = ldflag(rw, OFF_FP + tid * 8);
  const cf pm = {1.f, -1.f};
  v4u r1[4][8], w2[14][4];
  const int l16 = lane * 16, w2vo_ = OFF_W2P + (ct * 56 + 14 * wv) * 4096 + l16;
  int w2vo = w2vo_;
  const int rot2 = (4 * sub + 2 * ct) % 14;
  int k2o[14];
#pragma unroll
  for (int k = 0; k < 14; k++) k2o[k] = (k + rot2 >= 14 ? k + rot2 - 14 : k + rot2) * 1024;
#define LW1(lo, hi) do { _Pragma("unroll") for (int n_ = lo; n_ < hi; n_++) r1[n_ >> 3][n_ & 7] = ldp(n_ < 16 ? rg : ru, w1off, (((n_ >> 3) & 1) * 224 + (n_ & 7)) * F_ * 2); } while (0)
#define BG(lo, hi) do { _Pragma("unroll") for (int n_ = lo; n_ < hi; n_++) w2[n_ >> 2][n_ & 3] = ldc(rw, w2vo + (n_ & 3) * 1024, 4 * k2o[n_ >> 2]); } while (0)
#define PINW2(lo, hi) do { _Pragma("unroll") for (int n_ = lo; n_ < hi; n_++) asm volatile("" : "+a"(w2[n_ >> 2][n_ & 3])); } while (0)
#define IL(cnt, nv) do { _Pragma("unroll") for (int n_ = 0; n_ < cnt; n_++) { __builtin_amdgcn_sched_group_barrier(0x20, 1, 0); __builtin_amdgcn_sched_group_barrier(0x82, nv, 0); } SB(); CFENCE(); } while (0)
#define ILS(cnt, nv) do { _Pragma("unroll") for (int n_ = 0; n_ < cnt; n_++) { __builtin_amdgcn_sched_group_barrier(0x82, nv, 0); __builtin_amdgcn_sched_group_barrier(0x40, 1, 0); } SB(); CFENCE(); } while (0)
#define W1ST(r) do { _Pragma("unroll") for (int i_ = 0; i_ < 8; i_++) *(v4u*)(smem + w1lds[(r) & 1] + (2 * ((r) >> 1) + 4 * (i_ >> 1) + (i_ & 1)) * 16) = col8(r1[r], i_); } while (0)
  SB();
#pragma unroll
  for (int i = 0; i < 7; i++) z[i] = FMA2(z[i], mp[i].yy, mp[i].xx);
  fft7<false>(z);
  sc[e1w] = z[0];
#pragma unroll
  for (int i = 1; i < 7; i++) sc[i * 72 + e1w] = cmul(z[i], ta[i]);
  stc(pko[0], rw, pdst[0]); stc(pko[1], rw, pdst[1]);
  ILS(2, 24);
#pragma unroll
  for (int i = 0; i < 8; i++) z[i] = sc[e1r + 4 * i];
  SB();
  fft8<false>(z);
#pragma unroll
  for (int i = 0; i < 8; i++) {
    sc[e2a[i >> 2] + 8 * i] = i ? cmul(z[i], tb[i]) : z[0];
  }
  CFENCE();
#pragma unroll
  for (int c = 0; c < 4; c++) {
    const f32x4 v = *(const f32x4*)(sc + e2b[c]);
    z[2 * c] = cf{v[0], v[1]}; z[2 * c + 1] = cf{v[2], v[3]};
  }
  fft8<false>(z);
#pragma unroll
  for (int i = 0; i < 8; i++) zs[e3 + 56 * i] = z[i];
  if (RARE(flag_miss(fp, tag))) pollflag(rw, OFF_FP + tid * 8, tag);
  BAR();
  {
#pragma unroll
    for (int j = 0; j < 4; j++) {
      const cf zk = zs[j * 448 + kkv], zn = zs[j * 448 + knv];
      z[2 * j] = FMA2(zn, pm, zk); z[2 * j + 1] = FMA2(zk.yx, pm, zn.yx);
    }
    fft8<false>(z);
    unsigned o[8];
    o[0] = pk_bf16(z[0].x, z[0].y);
#pragma unroll
    for (int i = 1; i < 8; i++) { const cf v = cmul(z[i], tc[i]); o[i] = pk_bf16(v.x, v.y); }
    SB(); CFENCE();
    stc(v4u{o[0], o[1], o[2], o[3]}, rw, kk * 32, OFF_Z1 + m * ZST);
    stc(v4u{o[4], o[5], o[6], o[7]}, rw, kk * 32 + 16, OFF_Z1 + m * ZST);
  }
  SB();
  asm volatile("s_waitcnt vmcnt(2)" ::: "memory");
  BAR();
  stflag(rw, tid == 0 ? OFF_FQ + b * 8 : OOB, tag);
  __builtin_amdgcn_raw_buffer_store_b32(tag, rw, b == 0 && tid == 0 ? OFF_E : OOB, 0, SC1);
  v4u in0, in1;
  int hoff, hoff3, so_x1, lo_h2[2];
  auto late_setup = [&]() __attribute__((always_inline)) {
    hoff = (16 * p + 4 * wv + ((lane >> 4) & 1)) * 57344 + (7 * q + (lane >> 5)) * 256 + (lane & 15) * 16 + OFF_H;
    hoff3 = lane < 32 ? hoff + 1536 : OOB;
    PINV(hoff); PINV(hoff3);
    const float scale = 1.0f / (2.0f * 2048.0f * 448.0f);
#pragma unroll
    for (int i = 1; i < 8; i++) { tc[i] *= bc(scale); PINV(tc[i]); }
    so_x1 = OFF_X1 + (8 * m + 2 * wv + (lane >> 5)) * 896 + (lane & 31) * 4;
    PINV(so_x1);
#pragma unroll
    for (int r = 0; r < 2; r++) {
      const int e = tid + 256 * r < 448 ? tid + 256 * r : 447;
      lo_h2[r] = ((8 + (e & 7)) * 456 + (e >> 3) * 8) * 2;
      PINV(so_h2[r]); PINV(lo_h2[r]);
    }
    SB();
  };
  const int fro = OFF_FR + (lane < 28 ? lane : 27) * 8;
  if (!RARE(!p2_on)) {
    LW1(0, 4); SB();
    mk_pow<8>(ua, m2, 1.0f / 256.0f);
    SB(); LW1(4, 8); SB();
    mk_pow<8>(ub, kap, 1.0f / 32.0f);
    SB(); LW1(8, 12); SB();
    int ea, eb0;
    {
      const int m3 = m2 & 3, n3 = m2 >> 2;
      ea = (m3 & 1) | (kb << 1) | (((kap & 1) ^ (m3 >> 1)) << 2) | (((kap >> 1) ^ (n3 & 1)) << 3) | ((m3 >> 1) << 5) | (n3 << 6);
      const int k1 = lane & 7, k2b0 = (lane >> 3) & 1;
      eb0 = (kap & 1) | ((k1 >> 2) << 1) | (((k1 & 1) ^ (kap >> 1)) << 2) | (((k1 >> 1) & 1) << 3) | (k2b0 << 4) | ((kap >> 1) << 5) | (wv << 9);
    }
    int goff = OFF_Z2 + (32 * kap + m2) * ZST + kc * 32 + kb * 16;
    int goff1 = OFF_Z1 + (32 * kap + m2) * ZST + kc * 32 + kb * 16;
    PINV(ea); PINV(eb0); PINV(goff); PINV(goff1);
    SB();
    in0 = ldc(rw, goff1); in1 = ldc(rw, goff1 + 128 * ZST);
    SB();
    late_setup();
    repoll2(rw, goff1, goff1 + 128 * ZST, in0, in1);
    const v4u w0 = ldc(rw, tid * ZST, OFF_WT + kc * 32), w1 = ldc(rw, tid * ZST + 16, OFF_WT + kc * 32);
    SB();
    unsigned u[8] = {in0[0], in0[1], in0[2], in0[3], in1[0], in1[1], in1[2], in1[3]};
    tr4(u[0], u[1], u[2], u[3]); tr4(u[4], u[5], u[6], u[7]);
#pragma unroll
    for (int i = 0; i < 8; i++) z[i] = unpk(u[i]);
    fft8<false>(z);
#pragma unroll
    for (int i = 0; i < 8; i++) lds[ea + ((i & 1) << 4) + ((i >> 1) << 9)] = i ? cmul(z[i], ua[i]) : z[0];
    BAR();
#pragma unroll
    for (int i = 0; i < 8; i++) z[i] = lds[(eb0 ^ ((i & 1) << 3)) + (i << 6)];
    SB();
    LW1(12, 20);
    fft8<false>(z);
#pragma unroll
    for (int i = 1; i < 8; i++) z[i] = cmul(z[i], ub[i]);
    tr4c(z); tr4c(z + 4);
#pragma unroll
    for (int i = 0; i < 8; i++) W[i] = unpk(i < 4 ? w0[i] : w1[i - 4]);
    fft4<false>(z); fft4<false>(z + 4);
#pragma unroll
    for (int i = 0; i < 8; i++) z[i] = cmul(z[i], W[i]);
    fft4<true>(z); fft4<true>(z + 4);
    tr4c(z); tr4c(z + 4);
#pragma unroll
    for (int i = 1; i < 8; i++) z[i] = cmulc(z[i], ub[i]);
    fft8<true>(z);
#pragma unroll
    for (int i = 0; i < 8; i++) lds[(eb0 ^ ((i & 1) << 3)) + (i << 6)] = z[i];
    IL(8, 26);
    BAR();
#pragma unroll
    for (int i = 0; i < 8; i++) z[i] = lds[ea + ((i & 1) << 4) + ((i >> 1) << 9)];
    SB();
#pragma unroll
    for (int i = 1; i < 8; i++) z[i] = cmulc(z[i], ua[i]);
    fft8<true>(z);
#pragma unroll
    for (int i = 0; i < 8; i++) u[i] = pk_bf16(z[i].x, z[i].y);
    tr4(u[0], u[1], u[2], u[3]); tr4(u[4], u[5], u[6], u[7]);
    SB();
    stc(v4u{u[0], u[1], u[2], u[3]}, rw, goff);
    stc(v4u{u[4], u[5], u[6], u[7]}, rw, goff + 128 * ZST);
  } else {
    const int cg = b - 225;
    const rsrc_t rm2 = mk_rsrc(wmod, cg < 28 ? D_ * 2688 * 2 : 0);
    v4u w3[14]; unsigned c3[14];
#pragma unroll
    for (int i = 0; i < 14; i++) {
      w3[i] = ldp(rm2, ((tid >> 3) * 2688 + 896 + 64 * cg + 8 * (tid & 7)) * 2, i * 32 * 2688 * 2);
      c3[i] = __builtin_amdgcn_raw_buffer_load_b16(rc, (tid >> 3) * 2, i * 64, 0);
    }
    SB();
    late_setup();
    float acc[8];
#pragma unroll
    for (int j = 0; j < 8; j++) acc[j] = 0.f;
#pragma unroll
    for (int i = 0; i < 14; i++) gemv8(acc, c3[i], w3[i]);
    const float v = rbf(rbf(gemv_reduce(acc, (float*)lds, tid)) + (cg >= 14 && cg < 21 ? 1.0f : 0.0f));
    __builtin_amdgcn_raw_buffer_store_b32(f2u(v), rw, tid < 64 && cg < 28 ? OFF_M2 + ((cg % 7) * 64 + tid) * 16 + (cg / 7) * 4 : OOB, 0, SC1);
    DRAIN();
    stflag(rw, tid == 0 && cg < 28 ? OFF_FR + cg * 8 : OOB, tag);
    LW1(0, 20);
  }
  SB();
  const f256 fq = ld256(rw, OFF_FQ, lane);
  LW1(20, 32);
  SB();
  unsigned x1u[7];
  in0 = ldc(rw, OFF_Z2 + zoff); in1 = ldc(rw, OFF_Z2 + zoff + 16);
  SB();
  W1ST(0); W1ST(1);
  PINV(w2vo);
#pragma unroll
  for (int k = 0; k < 14; k++) asm volatile("" : "+s"(k2o[k]));
  SB();
  if (RARE(miss256(fq, tag))) poll256(rw, OFF_FQ, lane, tag);
  repoll2(rw, OFF_Z2 + zoff, OFF_Z2 + zoff + 16, in0, in1);
  int m2o = OFF_M2 + ml_ * 16;
  PINV(m2o);
  const v2u fm = ldflag(rw, fro);
  stc(ones, rw, so_h2[0]); stc(ones, rw, so_h2[1]);
  SB();
  {
    BG(0, 3);
    z[0] = unpk(in0[0]) * bc(1.0f / (2.0f * 2048.0f * 448.0f));
#pragma unroll
    for (int i = 1; i < 8; i++) z[i] = cmulc(unpk(i < 4 ? in0[i] : in1[i - 4]), tc[i]);
    fft8<true>(z);
    const cf keep = {1.f, (kk == 0 || kk == 224) ? 0.f : 1.f};
#pragma unroll
    for (int j = 0; j < 4; j++) {
      const cf ya = z[2 * j] * keep, yb = z[2 * j + 1] * keep;
      zs[j * 448 + kkv] = addpi(ya, yb);
      zs[j * 448 + knv] = FMA2(ya, pm, yb.yx);
    }
    IL(3, 25);
    BAR();
#pragma unroll
    for (int i = 0; i < 8; i++) z[i] = zs[e3 + 56 * i];
    if (RARE(flag_miss(fm, tag))) pollflag(rw, fro, tag);
    v4u ml[7];
#pragma unroll
    for (int i = 0; i < 7; i++) ml[i] = ldc(rw, m2o, i * 1024);
    SB();
    BG(3, 5);
    fft8<true>(z);
#pragma unroll
    for (int i = 1; i < 8; i++) z[i] = cmulc(z[i], tb[i]);
#pragma unroll
    for (int c = 0; c < 4; c++)
      *(f32x4*)(sc + e2b[c]) = f32x4{z[2 * c].x, z[2 * c].y, z[2 * c + 1].x, z[2 * c + 1].y};
    IL(2, 24);
#pragma unroll
    for (int i = 0; i < 8; i++) {
      z[i] = sc[e2a[i >> 2] + 8 * i];
    }
    SB();
    BG(5, 7);
    fft8<true>(z);
#pragma unroll
    for (int i = 0; i < 8; i++) sc[e1r + 4 * i] = z[i];
    IL(2, 18);
#pragma unroll
    for (int i = 0; i < 7; i++) z[i] = sc[i * 72 + e1w];
    SB();
    BG(7, 10);
#pragma unroll
    for (int i = 1; i < 7; i++) z[i] = cmulc(z[i], ta[i]);
    fft7<true>(z);
    unsigned char* stg = (unsigned char*)(lds + 4 * 512 + 4 * 448);
    unsigned short* stw = (unsigned short*)stg + (2 * wv) * 456 + ml_;
#pragma unroll
    for (int i = 0; i < 7; i++) {
      PINV(ml[i]);
      const cf t = FMA2(z[i], bc(u2f(ml[i][0])), xr[i]);
      x1u[i] = pk_bf16(t.x, t.y);
      z[i] = unpk(x1u[i]);
    }
    IL(3, 24);
    BG(10, 14);
    ln2rows(z);
#pragma unroll
    for (int i = 0; i < 7; i++) {
      const cf h = FMA2(z[i], bc(u2f(ml[i][2])), bc(u2f(ml[i][1])));
      const unsigned u = pk_bf16(h.x, h.y);
      stw[8 * 456 + 64 * i] = (unsigned short)u;
      stw[9 * 456 + 64 * i] = (unsigned short)(u >> 16);
    }
    IL(4, 24);
    BAR();
    const v4u o0 = *(const v4u*)(stg + lo_h2[0]), o1 = *(const v4u*)(stg + lo_h2[1]);
    stc(o0, rw, so_h2[0]); stc(o1, rw, so_h2[1]);
    SB();
    asm volatile("s_barrier" ::: "memory");
    stflag(rw, tid == 0 ? OFF_FD + m * 128 : OOB, tag);
    const unsigned sel = lane < 32 ? 0x05040100u : 0x07060302u;
#pragma unroll
    for (int i = 0; i < 7; i++) {
      unsigned a = x1u[i], c = a;
      swap32(a, c);
      __builtin_amdgcn_raw_buffer_store_b32(__builtin_amdgcn_perm(c, a, sel), rw, so_x1 + 128 * i, 0, SC1);
    }
  }
  SB();
  BG(14, 24);
  W1ST(2); W1ST(3);
#pragma unroll
  for (int n_ = 0; n_ < 10; n_++) { __builtin_amdgcn_sched_group_barrier(0x20, 1, 0); __builtin_amdgcn_sched_group_barrier(0x82, 8, 0); }
  SB();
  typedef const __attribute__((address_space(3))) v4u* lptr;
  unsigned ab0 = (unsigned)(size_t)(const __attribute__((address_space(3))) unsigned char*)smem + l16, ab1 = ab0 + 49 * W1_FR;
  int feo = lane == 0 ? OFF_FE + ((p * 4 + wv) * 64 + q) * 128 : OOB;
  int fpo = OFF_FE + ((xc * 4 + sub) * 64 + 8 * wv + (lane & 7)) * 128;
  int fdo = OFF_FD + (32 * p + 8 * wv + (lane & 7)) * 128;
  const int vt = (16 * xc + 4 * sub + (wv >> 1)) * 16 + (lane & 15), ncol = ct * 64 + 32 * (wv & 1) + 8 * (lane >> 4);
  int x1o = OFF_X1 + vt * 896 + ncol * 2, oo = (((vt >> 3) + 256 * (vt & 7)) * D_ + ncol) * 2;
  int fxo = (lane & 8) ? OFF_FD + (32 * xc + 8 * sub + (lane & 7)) * 128 + 64 : fpo;
  int gbo = OFF_M2 + ncol * 16 + 12;
  PINV(gbo);
  int h2v[4], hv[4], k1o[14];
#pragma unroll
  for (int t = 0; t < 4; t++) {
    h2v[t] = OFF_H2 + (16 * p + 4 * wv + t) * 14336 + l16;
    hv[t] = OFF_H + (16 * xc + 4 * sub + t) * 57344 + 14 * wv * 1024 + l16;
    PINV(h2v[t]); PINV(hv[t]);
  }
#pragma unroll
  for (int k = 0; k < 14; k++) {
    k1o[k] = (k + rot1 >= 14 ? k + rot1 - 14 : k + rot1) * 1024;
    asm volatile("" : "+s"(k1o[k]));
  }
  PINV(ab0); PINV(ab1); PINV(feo); PINV(fpo); PINV(fdo); PINV(x1o); PINV(oo); PINV(fxo);
  SB();
  pollflag(rw, fdo, tag);
  PINW2(0, 24);
  v4u bt[28][2], hA[14][2], hB[14][2];
#define LDB(s) do { bt[s][0] = ldc(rw, h2v[2 * ((s) / 14)], k1o[(s) % 14]); bt[s][1] = ldc(rw, h2v[2 * ((s) / 14) + 1], k1o[(s) % 14]); } while (0)
#define LDH(h, half, k) do { h[k][0] = ldc(rw, hv[2 * (half)], k2o[k]); h[k][1] = ldc(rw, hv[2 * (half) + 1], k2o[k]); } while (0)
#pragma unroll
  for (int s = 0; s < RING; s++) { LDB(s); SB(); }
  BAR();
  stflag(rw, tid == 0 ? OFF_FD + m * 128 + 64 : OOB, tag);
  v4u af[2][7];
#define AFRAG(f) (*(lptr)(size_t)((f) < 49 ? ab0 + (f) * W1_FR : ab1 + ((f) - 49) * W1_FR))
#pragma unroll
  for (int T = 0; T < 7; T++) af[0][T] = AFRAG(T);
  SB();
  f32x4 aA[7][2], aB[7][2];
  v2u fA = {0u, 0u};
  auto redo1 = [&](f32x4 (*a)[2], int half) __attribute__((always_inline)) {
    pollflag(rw, fdo + 64, tag);
#pragma unroll
    for (int T = 0; T < 7; T++) a[T][0] = a[T][1] = f32x4{0.f, 0.f, 0.f, 0.f};
#pragma unroll 1
    for (int ks = 0; ks < 14; ks++) {
      const int ko = (ks + rot1 >= 14 ? ks + rot1 - 14 : ks + rot1) * 1024;
      const v4u b0 = ldc(rw, h2v[2 * half], ko), b1 = ldc(rw, h2v[2 * half + 1], ko);
#pragma unroll
      for (int T = 0; T < 7; T++) {
        const v4u w = *(lptr)(size_t)(ab0 + (ks * 7 + T) * W1_FR);
        MFMA(a[T][0], w, b0); MFMA(a[T][1], w, b1);
      }
    }
    SB(); asm volatile("s_nop 7" ::: "memory"); SB();
  };
  sfor<0, 28>([&](auto s_) __attribute__((always_inline)) {
    constexpr int s = decltype(s_)::v, ks = s % 14;
    if constexpr (s == 14) {
      SB(); asm volatile("s_nop 7" ::: "memory"); SB();
      if (RARE(any_nan(aA[0][0][0], aA[0][1][0]))) redo1(aA, 0);
      SB(); asm volatile("s_nop 1" ::: "memory"); SB();
    }
    if constexpr (s == 20) {
      asm volatile("s_waitcnt vmcnt(16)" ::: "memory");
      stflag(rw, feo, tag);
      SB();
    }
    sfor<0, 7>([&](auto t_) __attribute__((always_inline)) {
      constexpr int T = decltype(t_)::v;
      if constexpr (s < 27) af[(s + 1) & 1][T] = AFRAG(((s + 1) % 14) * 7 + T);
      if constexpr (s < 14) {
        if constexpr (ks == 0) { MFMA0(aA[T][0], af[s & 1][T], bt[s][0]); MFMA0(aA[T][1], af[s & 1][T], bt[s][1]); }
        else { MFMA(aA[T][0], af[s & 1][T], bt[s][0]); MFMA(aA[T][1], af[s & 1][T], bt[s][1]); }
      } else {
        if constexpr (ks == 0) { MFMA0(aB[T][0], af[s & 1][T], bt[s][0]); MFMA0(aB[T][1], af[s & 1][T], bt[s][1]); }
        else { MFMA(aB[T][0], af[s & 1][T], bt[s][0]); MFMA(aB[T][1], af[s & 1][T], bt[s][1]); }
      }
      if constexpr (s + RING < 28 && T == 3) LDB(s + RING);
      if constexpr (s >= 2 && s < 14 && T == 0) BG(22 + s, 23 + s);
      if constexpr (s >= 16 && s < 26 && (T == 0 || T == 5)) BG(36 + 2 * (s - 16) + (T == 5), 37 + 2 * (s - 16) + (T == 5));
      if constexpr ((s == 14 || s == 15) && (T == 1 || T == 4)) g1_epi(aA, 2 * (s - 14) + (T == 4), rw, hoff, hoff3);
      SB();
    });
    if constexpr (s == 26) { fA = ldflag(rw, fxo); SB(); }
  });
  const bool g2_on = ix < 28;
  if (RARE(g2_on && flag_miss(fA, tag))) pollflag(rw, fxo, tag);
  CFENCE();
#pragma unroll
  for (int k = 0; k < 6; k++) { LDH(hA, 0, k); SB(); }
  if (RARE(any_nan(aB[0][0][0], aB[0][1][0]))) redo1(aB, 1);
#pragma unroll
  for (int gi = 0; gi < 4; gi++) g1_epi(aB, gi, rw, hoff + 2 * 57344, hoff3 + 2 * 57344);
  SB();
  if (RARE(!g2_on)) {
    DRAIN();
    stflag(rw, feo + 32 * 128, tag);
    return;
  }
  PINW2(24, 56);
  SB(); asm volatile("s_nop 1" ::: "memory"); SB();
  f32x4 acc[4][2];
  v4u xv[2];
  cf gb[4];
  sfor<0, 14>([&](auto k_) __attribute__((always_inline)) {
    constexpr int ks = decltype(k_)::v;
    if constexpr (ks == 6) {
      asm volatile("s_waitcnt vmcnt(10)" ::: "memory");
      stflag(rw, feo + 32 * 128, tag);
      SB();
    }
#pragma unroll
    for (int i = 0; i < 4; i++) {
      if (ks == 0) { MFMA0_A(acc[i][0], w2[ks][i], hA[ks][0]); MFMA0_A(acc[i][1], w2[ks][i], hA[ks][1]); }
      else { MFMA_A(acc[i][0], w2[ks][i], hA[ks][0]); MFMA_A(acc[i][1], w2[ks][i], hA[ks][1]); }
      if constexpr (ks >= 1 && ks < 6) { if (i == 1) LDH(hA, 0, 5 + ks); }
      if constexpr (ks == 6 || ks == 7) { if ((i & 1) && 11 + 2 * (ks - 6) + (i >> 1) < 14) LDH(hA, 0, 11 + 2 * (ks - 6) + (i >> 1)); }
      if constexpr (ks == 8) { if (i == 1) { xv[0] = ldc(rw, x1o); xv[1] = ldc(rw, x1o + 32 * 896); } }
      if constexpr (ks == 9) gb[i] = cf{u2f(__builtin_amdgcn_raw_buffer_load_b32(rw, gbo + 32 * i, 0, SC1)), u2f(__builtin_amdgcn_raw_buffer_load_b32(rw, gbo + 32 * i + 16, 0, SC1))};
      SB();
    }
  });
  const v2u fB = ldflag(rw, fpo + 32 * 128);
  SB(); asm volatile("s_nop 7" ::: "memory"); SB();
  const rsrc_t ro = mk_rsrc(out, S_ * D_ * 2);
  auto put = [&](unsigned char* base) __attribute__((always_inline)) {
#pragma unroll
    for (int tt = 0; tt < 2; tt++)
#pragma unroll
      for (int i = 0; i < 4; i++) *(f32x4*)(base + ((wv * 2 + tt) * 4 + i) * 1024 + l16) = acc[i][tt];
  };
  const int fro2 = (wv >> 1) * 4096 + ((2 * (wv & 1) + (lane >> 5)) * 16 + (lane & 15)) * 16 + ((lane >> 4) & 1) * 8;
  auto fin = [&](const unsigned char* base, v4u x1, int off) __attribute__((always_inline)) {
    cf sum[4];
#pragma unroll
    for (int i = 0; i < 4; i++) {
      const unsigned char* pp = base + i * 1024 + fro2;
      sum[i] = (*(const cf*)pp + *(const cf*)(pp + 8192)) + (*(const cf*)(pp + 16384) + *(const cf*)(pp + 24576));
    }
    const cf o[4] = {cf{sum[0].x, sum[1].x}, cf{sum[2].x, sum[3].x}, cf{sum[0].y, sum[1].y}, cf{sum[2].y, sum[3].y}};
    v4u ou;
#pragma unroll
    for (int e = 0; e < 4; e++) {
      const cf t = FMA2(o[e], gb[e], unpk(x1[e]));
      ou[e] = pk_bf16(t.x, t.y);
    }
    __builtin_amdgcn_raw_buffer_store_b128(ou, ro, off, 0, 0);
  };
  put(smem + LDS_SCR);
  SB();
  if (RARE(flag_miss(fB, tag))) pollflag(rw, fpo + 32 * 128, tag);
  CFENCE();
#pragma unroll
  for (int k = 0; k < 5; k++) { LDH(hB, 1, k); SB(); }
  BAR();
  LDH(hB, 1, 5); LDH(hB, 1, 6); LDH(hB, 1, 7); LDH(hB, 1, 8);
  fin(smem + LDS_SCR, xv[0], oo);
  __builtin_amdgcn_sched_group_barrier(0x100, 16, 0);
#pragma unroll
  for (int n_ = 0; n_ < 8; n_++) { __builtin_amdgcn_sched_group_barrier(0x20, 1, 0); __builtin_amdgcn_sched_group_barrier(0x2, 8, 0); }
  SB(); asm volatile("s_nop 1" ::: "memory"); SB();
#pragma unroll
  for (int ks = 0; ks < 14; ks++) {
#pragma unroll
    for (int i = 0; i < 4; i++) {
      if (ks == 0) { MFMA0_A(acc[i][0], w2[ks][i], hB[ks][0]); MFMA0_A(acc[i][1], w2[ks][i], hB[ks][1]); }
      else { MFMA_A(acc[i][0], w2[ks][i], hB[ks][0]); MFMA_A(acc[i][1], w2[ks][i], hB[ks][1]); }
      if (ks < 5 && i == 1) LDH(hB, 1, 9 + ks);
      SB();
    }
  }
  SB(); asm volatile("s_nop 7" ::: "memory"); SB();
  put(smem);
  BAR();
  fin(smem, xv[1], oo + 4 * D_ * 2);
}

extern "C" void kernel_launch(void* const* d_in, const int* in_sizes, int n_in, void* d_out, int out_size, void* d_ws,
                              size_t ws_size, hipStream_t stream) {
  typedef const unsigned short* cp;
  (void)hipFuncSetAttribute((const void*)k1, hipFuncAttributeMaxDynamicSharedMemorySize, LDS_BYTES);
  k1<<<256, 256, LDS_BYTES, stream>>>((cp)d_in[0], (cp)d_in[1], (cp)d_in[2], (cp)d_in[4], (cp)d_in[5], (cp)d_in[6], (cp)d_in[7],
                                      (cp)d_in[8], (unsigned char*)d_ws, (unsigned short*)d_out);
}
```
